```python
import torch

def _softmax(x, axis=-1):
    return torch.softmax(x, dim=axis)
def _gelu(x):
    return torch.nn.functional.gelu(x, approximate="tanh")
def _silu(x):
    return torch.nn.functional.silu(x)
def _sigmoid(x):
    return torch.sigmoid(x)
def _layernorm(x, axis=-1, eps=1e-5):
    m = x.mean(dim=axis, keepdim=True)
    v = x.var(dim=axis, keepdim=True, unbiased=False)
    return (x - m) / torch.sqrt(v + eps)
def _scatter_add(tgt, idx, src, axis=0):
    return tgt.clone().index_add_(axis, idx, src)
def _segment_sum(src, seg, n):
    out = torch.zeros((n,) + src.shape[1:], dtype=src.dtype)
    return out.index_add_(0, seg, src)
def _pad(x, pw):
    flat = tuple(v for pair in reversed(pw) for v in pair)
    return torch.nn.functional.pad(x, flat)

kernel_name = 'parallel_sum__ssm_conv_s4__glu_gated_geglu__prompt'

def setup_inputs():

    _w = (lambda a: (lambda t: t.to(torch.bfloat16) if t.is_floating_point() else t)(torch.as_tensor(a)))
    def _A_setup():
        B, L, D, N = 1, 2048, 448, 256
        A_re = -(lambda _x: torch.exp(torch.as_tensor(_x)))((lambda *s: torch.randn(*s, dtype=torch.bfloat16) * 6.4)(D, N))
        A_im = torch.pi * (lambda *s: torch.randn(*s, dtype=torch.bfloat16) * 0.05)(D, N)
        B_re = (lambda *s: torch.randn(*s, dtype=torch.bfloat16) * 0.1)(D, N)
        B_im = (lambda *s: torch.randn(*s, dtype=torch.bfloat16) * 0.1)(D, N)
        C_re = (lambda *s: torch.randn(*s, dtype=torch.bfloat16) * 0.1)(D, N)
        C_im = (lambda *s: torch.randn(*s, dtype=torch.bfloat16) * 0.1)(D, N)
        log_dt = (lambda *s: torch.randn(*s, dtype=torch.bfloat16) * 0.05)(D) * 0.5 - 4.0
        Dskip = (lambda *s: torch.randn(*s, dtype=torch.bfloat16) * 0.05)(D)
        return {
            "u": _w((lambda *s: torch.randn(*s, dtype=torch.bfloat16) * 0.05)(B, L, D)),
            "A_re": _w(A_re),
            "A_im": _w(A_im),
            "B_re": _w(B_re),
            "B_im": _w(B_im),
            "C_re": _w(C_re),
            "C_im": _w(C_im),
            "log_dt": _w(log_dt),
            "Dskip": _w(Dskip),
        }
    _A_in = _A_setup()

    def _B_setup():
        B, S, Dm, Df = 1, 2048, 448, 1792
        return {
            "x": _w((lambda *s: torch.randn(*s, dtype=torch.bfloat16) * 0.05)(B, S, Dm)),
            "w_gate": _w((lambda *s: torch.randn(*s, dtype=torch.bfloat16) * 0.4)(Dm, Df) * (Dm ** -0.5)),
            "w_up": _w((lambda *s: torch.randn(*s, dtype=torch.bfloat16) * 0.4)(Dm, Df) * (Dm ** -0.5)),
            "w_down": _w((lambda *s: torch.randn(*s, dtype=torch.bfloat16) * 0.4)(Df, Dm) * (Df ** -0.5)),
        }
    _B_in = _B_setup()

    return {
        "A_u": _A_in["u"],
        "A_A_re": _A_in["A_re"],
        "A_A_im": _A_in["A_im"],
        "A_B_re": _A_in["B_re"],
        "A_B_im": _A_in["B_im"],
        "A_C_re": _A_in["C_re"],
        "A_C_im": _A_in["C_im"],
        "A_log_dt": _A_in["log_dt"],
        "A_Dskip": _A_in["Dskip"],
        "B_x": _B_in["x"],
        "B_w_gate": _B_in["w_gate"],
        "B_w_up": _B_in["w_up"],
        "B_w_down": _B_in["w_down"],
    }

def reference(A_u, A_A_re, A_A_im, A_B_re, A_B_im, A_C_re, A_C_im, A_log_dt, A_Dskip, B_x, B_w_gate, B_w_up, B_w_down):
    def _A_ref(u, A_re, A_im, B_re, B_im, C_re, C_im, log_dt, Dskip):
        Bsz, L, D = u.shape
        dt = torch.exp(log_dt)[:, None]
        A = A_re + 1j * A_im
        Bc = B_re + 1j * B_im
        Cc = C_re + 1j * C_im
        logdA = dt * A
        dB = dt * Bc
        l = (lambda x, dt: x.to(dt))(torch.arange(L), A_re.dtype)
        P = torch.exp(logdA[:, :, None] * l[None, None, :])
        K = torch.einsum("dn,dnl,dn->dl", Cc, P, dB).real
        L2 = 2 * L
        Kp = _pad(K, ((0, 0), (0, L)))
        up = (lambda x, *d: x.permute(*d))(u, 0, 2, 1)
        up = _pad(up, ((0, 0), (0, 0), (0, L)))
        Kf = (lambda x, axes=(-2,-1): torch.fft.rfft2(x.float(), dim=axes))(Kp[:, None, :], axes=(-2, -1))
        Uf = (lambda x, axes=(-2,-1): torch.fft.rfft2(x.float(), dim=axes))(up[:, :, None, :], axes=(-2, -1))
        Yf = Uf * Kf[None, :, :, :]
        y = (lambda x, s, axes=(-2,-1): torch.fft.irfft2(x, s=s, dim=axes).to(torch.bfloat16))(Yf, (1, L2), axes=(-2, -1))[:, :, 0, :L]
        y = (lambda x, *d: x.permute(*d))(y, 0, 2, 1) + u * Dskip[None, None, :]
        return y

    def _B_ref(x, w_gate, w_up, w_down):
        g = torch.einsum("bsd,df->bsf", x, w_gate)
        u = torch.einsum("bsd,df->bsf", x, w_up)
        h = _gelu(g) * u
        return torch.einsum("bsf,fd->bsd", h, w_down)

    x = A_u
    xn = _layernorm(x, axis=-1)
    return x + _A_ref(xn, A_A_re, A_A_im, A_B_re, A_B_im, A_C_re, A_C_im, A_log_dt, A_Dskip) + _B_ref(xn, B_w_gate, B_w_up, B_w_down)
```

```cpp
#include <hip/hip_runtime.h>
#include <hip/hip_bf16.h>
#include <stdint.h>

typedef unsigned short u16;
static constexpr int L = 2048, D = 448, N = 256, DF = 1792;

__device__ __forceinline__ float bf2f(u16 v) { return __uint_as_float(((unsigned)v) << 16); }
__device__ __forceinline__ u16 f2bf(float f) {
  unsigned u = __float_as_uint(f);
  return (u16)((u + 0x7FFFu + ((u >> 16) & 1u)) >> 16);
}
__device__ __forceinline__ float rbf(float f) { return bf2f(f2bf(f)); }

__global__ void k_ln(const u16* x, u16* xn) {
  int l = blockIdx.x; int t = threadIdx.x;
  float v[7]; float s = 0;
  for (int i = 0; i < 7; ++i) { v[i] = bf2f(x[l * D + t + 64 * i]); s += v[i]; }
  for (int o = 32; o; o >>= 1) s += __shfl_xor(s, o);
  float mean = s / D; float q = 0;
  for (int i = 0; i < 7; ++i) { float d = v[i] - mean; q += d * d; }
  for (int o = 32; o; o >>= 1) q += __shfl_xor(q, o);
  float var = rbf(q / D); float mb = rbf(mean);
  float sd = rbf(sqrtf(rbf(var + 1e-5f)));
  for (int i = 0; i < 7; ++i) xn[l * D + t + 64 * i] = f2bf(rbf(v[i] - mb) / sd);
}

__global__ void k_kgen(const u16* Are, const u16* Aim, const u16* Bre, const u16* Bim, const u16* Cre, const u16* Cim,
                       const u16* logdt, float* K) {
  int d = blockIdx.x;
  __shared__ float sa[N], sb[N], cr[N], ci[N];
  float dt = rbf(expf(bf2f(logdt[d])));
  for (int n = threadIdx.x; n < N; n += blockDim.x) {
    sa[n] = dt * bf2f(Are[d * N + n]); sb[n] = dt * bf2f(Aim[d * N + n]);
    float br = dt * bf2f(Bre[d * N + n]), bi = dt * bf2f(Bim[d * N + n]);
    float c_r = bf2f(Cre[d * N + n]), c_i = bf2f(Cim[d * N + n]);
    cr[n] = c_r * br - c_i * bi; ci[n] = c_r * bi + c_i * br;
  }
  __syncthreads();
  for (int j = threadIdx.x; j < L; j += blockDim.x) {
    float t = rbf((float)j); float acc = 0;
    for (int n = 0; n < N; ++n) {
      float e = expf(sa[n] * t); float s, c; sincosf(sb[n] * t, &s, &c);
      acc += e * (cr[n] * c - ci[n] * s);
    }
    K[d * L + j] = acc;
  }
}

__global__ void k_conv(const u16* xn, const float* K, float* y) {
  int d = blockIdx.x;
  __shared__ float su[L], sk[L];
  for (int i = threadIdx.x; i < L; i += blockDim.x) { su[i] = bf2f(xn[i * D + d]); sk[i] = K[d * L + i]; }
  __syncthreads();
  for (int l = threadIdx.x; l < L; l += blockDim.x) {
    float acc = 0;
    for (int j = 0; j <= l; ++j) acc += sk[j] * su[l - j];
    y[l * D + d] = acc;
  }
}

__global__ void k_gemm1(const u16* xn, const u16* wg, const u16* wu, u16* h) {
  int l = blockIdx.x; __shared__ float sx[D];
  for (int i = threadIdx.x; i < D; i += blockDim.x) sx[i] = bf2f(xn[l * D + i]);
  __syncthreads();
  for (int f = threadIdx.x; f < DF; f += blockDim.x) {
    float g = 0, u = 0;
    for (int k = 0; k < D; ++k) { g += sx[k] * bf2f(wg[k * DF + f]); u += sx[k] * bf2f(wu[k * DF + f]); }
    g = rbf(g); u = rbf(u);
    float ge = 0.5f * g * (1.f + tanhf(0.7978845608028654f * (g + 0.044715f * g * g * g)));
    h[l * DF + f] = f2bf(rbf(ge) * u);
  }
}

__global__ void k_gemm2(const u16* h, const u16* wd, const u16* x, const u16* xn, const float* y, const u16* dskip, u16* out) {
  int l = blockIdx.x; __shared__ float sh[DF];
  for (int i = threadIdx.x; i < DF; i += blockDim.x) sh[i] = bf2f(h[l * DF + i]);
  __syncthreads();
  for (int d = threadIdx.x; d < D; d += blockDim.x) {
    float acc = 0;
    for (int k = 0; k < DF; ++k) acc += sh[k] * bf2f(wd[k * D + d]);
    float b = rbf(acc);
    float a = rbf(rbf(y[l * D + d]) + rbf(bf2f(xn[l * D + d]) * bf2f(dskip[d])));
    out[l * D + d] = f2bf(rbf(bf2f(x[l * D + d]) + a) + b);
  }
}

extern "C" void kernel_launch(void* const* d_in, const int* in_sizes, int n_in,
                              void* d_out, int out_size, void* d_ws, size_t ws_size,
                              hipStream_t stream) {
  const u16* x = (const u16*)d_in[0];
  char* ws = (char*)d_ws;
  u16* xn = (u16*)ws; float* K = (float*)(ws + (4 << 20)); float* y = (float*)(ws + (8 << 20)); u16* h = (u16*)(ws + (16 << 20));
  k_ln<<<L, 64, 0, stream>>>(x, xn);
  k_kgen<<<D, 256, 0, stream>>>((const u16*)d_in[1], (const u16*)d_in[2], (const u16*)d_in[3], (const u16*)d_in[4], (const u16*)d_in[5], (const u16*)d_in[6], (const u16*)d_in[7], K);
  k_conv<<<D, 256, 0, stream>>>(xn, K, y);
  k_gemm1<<<L, 256, 0, stream>>>(xn, (const u16*)d_in[10], (const u16*)d_in[11], h);
  k_gemm2<<<L, 256, 0, stream>>>(h, (const u16*)d_in[12], x, xn, y, (const u16*)d_in[8], (u16*)d_out);
}
```

```cpp
#include <hip/hip_runtime.h>
#include <stdint.h>

typedef unsigned short u16;
typedef unsigned int u32;
typedef unsigned long long u64;
typedef __attribute__((ext_vector_type(4))) u32 u32x4;
typedef __attribute__((ext_vector_type(2))) u32 u32x2;
typedef __attribute__((ext_vector_type(4))) float f32x4;
typedef __attribute__((ext_vector_type(16))) float f32x16;
typedef __attribute__((ext_vector_type(8))) __bf16 bf16x8;
typedef __attribute__((ext_vector_type(2))) __bf16 bf16x2;
typedef __attribute__((ext_vector_type(2))) float f32x2;
typedef __attribute__((ext_vector_type(4))) short s16x4;
#define LAS __attribute__((address_space(3)))
#define GAS __attribute__((address_space(1)))
#define AINL __attribute__((always_inline))

static constexpr int D = 448;
static constexpr size_t WS_EPOCH = 0;
static constexpr size_t WS_ST = 4096;
static constexpr size_t WS_CS = WS_ST + 2048 * 8;
static constexpr size_t WS_FX = WS_CS + 3584 * 8;
static constexpr size_t WS_FH = WS_FX + 4096;
static constexpr size_t WS_FW = WS_FH + 8192;
static constexpr size_t WS_FA = WS_FW + 4096;
static constexpr size_t WS_HF = 128 << 10;
static constexpr size_t WS_WD = WS_HF + (size_t)64 * 112 * 1024;
static constexpr size_t WS_XT = WS_WD + (size_t)8 * 112 * 1792;
static constexpr int XT_ROW = 2112;
static constexpr size_t WS_AT = WS_XT + (size_t)448 * XT_ROW * 2 + 4096;
static constexpr size_t WS_DUMMY = WS_AT + (size_t)448 * 4096;
static constexpr size_t WS_STAMP = 128 << 20;

static constexpr int RS = 32768;
static constexpr int AUX = 0;
static constexpr int RB = 32768;
static constexpr int L_ST = AUX + 31232;
static constexpr int L_ST2 = AUX + 31744;
static constexpr int L_CS = AUX + 512;
static constexpr int L_CSP = AUX + 1536;
static constexpr int L_FLG = AUX + 2048;
static constexpr int L_TAB = AUX + 2304;
static constexpr int L_KP = AUX + 5376;
static constexpr int KP_SZ = 3072;
static constexpr int L_KI = AUX + 5376;
static constexpr int KI_SZ = 4224, KI_CH = 8448;
static constexpr int KG_STR = 288;
static constexpr int L_U = RB + 3 * RS;
static constexpr int U_SZ = 7424;
static constexpr int L_XCH = RB;
static constexpr int PSTR = 240, PART = 64 * PSTR;
static constexpr int L_PART = RB + RS;
static constexpr int L_BASE = RB + 16384;
static constexpr int WB_UP = 15488;
static constexpr int LDS_BYTES = 163840;
static constexpr u32 CODE_LINES = 384;
static constexpr u32 SPIN_CAP = 1u << 16;

__device__ __forceinline__ float bf2f(u32 v) { return __uint_as_float(v << 16); }
__device__ __forceinline__ float bfhi(u32 v) { return __uint_as_float(v & 0xffff0000u); }
__device__ __forceinline__ u32 pk_bf16(float lo, float hi) {
  f32x2 v = {lo, hi}; bf16x2 b = __builtin_convertvector(v, bf16x2); return __builtin_bit_cast(u32, b);
}
__device__ __forceinline__ float rbf(float f) { return __uint_as_float(pk_bf16(f, f) << 16); }
template <int I> struct IC { static constexpr int value = I; };
template <int B, int E, class F> __device__ __forceinline__ void static_for(F&& f) {
  if constexpr (B < E) { f(IC<B>{}); static_for<B + 1, E>(f); }
}
#define WAITV(n) asm volatile("s_waitcnt vmcnt(" #n ")" ::: "memory")
#define BARRIER() asm volatile("s_waitcnt lgkmcnt(0)\n\ts_barrier" ::: "memory")
__device__ __forceinline__ u64 rtc() { return __builtin_amdgcn_s_memrealtime(); }

__device__ __forceinline__ void gld16(u32x4& d, u32 voff, const void* sb) {
  asm volatile("global_load_dwordx4 %0, %1, %2" : "=v"(d) : "v"(voff), "s"(sb) : "memory"); }
__device__ __forceinline__ void gld16_sc1(u32x4& d, u32 voff, const void* sb) {
  asm volatile("global_load_dwordx4 %0, %1, %2 sc1" : "=v"(d) : "v"(voff), "s"(sb) : "memory"); }
__device__ __forceinline__ void gld4(u32& d, u32 voff, const void* sb) {
  asm volatile("global_load_dword %0, %1, %2" : "=v"(d) : "v"(voff), "s"(sb) : "memory"); }
__device__ __forceinline__ void gld4_sc1(u32& d, u32 voff, const void* sb) {
  asm volatile("global_load_dword %0, %1, %2 sc1" : "=v"(d) : "v"(voff), "s"(sb) : "memory"); }
__device__ __forceinline__ void gld2(u32& d, u32 voff, const void* sb) {
  asm volatile("global_load_ushort %0, %1, %2" : "=v"(d) : "v"(voff), "s"(sb) : "memory"); }
template <int OFF> __device__ __forceinline__ void gld2o(u32& d, u32 voff, const void* sb) {
  asm volatile("global_load_ushort %0, %1, %2 offset:%c3" : "=v"(d) : "v"(voff), "s"(sb), "i"(OFF) : "memory"); }
template <int R, int OFF> __device__ __forceinline__ void gld16a_sc1(u32 voff, const void* sb) {
  asm volatile("global_load_dwordx4 a[%c2:%c3], %0, %1 offset:%c4 sc1" :: "v"(voff), "s"(sb), "i"(R), "i"(R + 3), "i"(OFF) : "memory"); }
template <int OFF> __device__ __forceinline__ void gst16o_sc1(u32 voff, u32x4 v, void* sb) {
  asm volatile("global_store_dwordx4 %0, %1, %2 offset:%c3 sc1\n\ts_nop 1" :: "v"(voff), "v"(v), "s"(sb), "i"(OFF) : "memory"); }
template <int OFF> __device__ __forceinline__ void dsw_tid(u32 v) { asm volatile("ds_write_addtid_b32 %0 offset:%c1" :: "v"(v), "i"(OFF) : "memory"); }
__device__ __forceinline__ void kg_read(u32x4 (&f)[4], u32 ad) {
  asm volatile("ds_read_b128 %0, %4\n\tds_read_b128 %1, %4 offset:64\n\tds_read_b128 %2, %4 offset:128\n\tds_read_b128 %3, %4 offset:192"
               : "=&v"(f[0]), "=&v"(f[1]), "=&v"(f[2]), "=&v"(f[3]) : "v"(ad) : "memory");
}
#define PINV(x) asm volatile("" : "+v"(x))
__device__ __forceinline__ void gst16_sc1(u32 voff, u32x4 v, void* sb) {
  asm volatile("global_store_dwordx4 %0, %1, %2 sc1\n\ts_nop 1" :: "v"(voff), "v"(v), "s"(sb) : "memory"); }
template <int OFF> __device__ __forceinline__ void gst8_sc1(u32 voff, u32x2 v, void* sb) {
  asm volatile("global_store_dwordx2 %0, %1, %2 offset:%3 sc1" :: "v"(voff), "v"(v), "s"(sb), "i"(OFF) : "memory"); }
__device__ __forceinline__ void gst4_sc1(u32 voff, u32 v, void* sb) {
  asm volatile("global_store_dword %0, %1, %2 sc1" :: "v"(voff), "v"(v), "s"(sb) : "memory"); }
__device__ __forceinline__ void setm0(u32 v) { asm volatile("s_mov_b32 m0, %0\n\ts_nop 0" :: "s"(v) : "memory"); }
template <int OFF> __device__ __forceinline__ void dma(u32 voff, const void* sb) {
  asm volatile("global_load_lds_dwordx4 %0, %1 offset:%2" :: "v"(voff), "s"(sb), "i"(OFF) : "memory"); }
template <int OFF> __device__ __forceinline__ void dma_sc1(u32 voff, const void* sb) {
  asm volatile("global_load_lds_dwordx4 %0, %1 offset:%2 sc1" :: "v"(voff), "s"(sb), "i"(OFF) : "memory"); }
__device__ __forceinline__ void poll_flags(u32 voff, const void* sb, u32 T) {
#pragma nounroll
  for (u32 s = 0; s < SPIN_CAP; ++s) {
    u32 v;
    asm volatile("global_load_dword %0, %1, %2 sc1\n\ts_waitcnt vmcnt(0)" : "=v"(v) : "v"(voff), "s"(sb) : "memory");
    if (__all(v == T)) break;
    __builtin_amdgcn_s_sleep(2);
  }
}
__device__ __forceinline__ u32x2 trread(LAS const char* p) {
  return __builtin_bit_cast(u32x2, __builtin_amdgcn_ds_read_tr16_b64_v4i16((LAS s16x4*)p));
}
__device__ __forceinline__ void MFMA32(f32x16& acc, const u32x4& A, const u32x4& B) { asm volatile("v_mfma_f32_32x32x16_bf16 %0, %1, %2, %0" : "+v"(acc) : "v"(A), "v"(B)); }
__device__ __forceinline__ void MFMA32_0(f32x16& acc, const u32x4& A, const u32x4& B) { asm volatile("v_mfma_f32_32x32x16_bf16 %0, %1, %2, 0" : "=&v"(acc) : "v"(A), "v"(B)); }
__device__ __forceinline__ void MFMA16(f32x4& acc, const u32x4& A, const u32x4& B) { asm volatile("v_mfma_f32_16x16x32_bf16 %0, %1, %2, %0" : "+v"(acc) : "v"(A), "v"(B)); }
__device__ __forceinline__ void MFMA16_0(f32x4& acc, const u32x4& A, const u32x4& B) { asm volatile("v_mfma_f32_16x16x32_bf16 %0, %1, %2, 0" : "=&v"(acc) : "v"(A), "v"(B)); }
template <int NW, int R, bool FIRST> __device__ __forceinline__ void g2_step(f32x16& o0, f32x16& o1, f32x16& o2, f32x16& o3) {
  if constexpr (FIRST)
    asm volatile("s_waitcnt vmcnt(%c4)\n\tv_mfma_f32_32x32x16_bf16 %0, a[%c5:%c6], a[%c7:%c8], 0\n\tv_mfma_f32_32x32x16_bf16 %1, a[%c5:%c6], a[%c11:%c12], 0\n\t"
                 "v_mfma_f32_32x32x16_bf16 %2, a[%c9:%c10], a[%c7:%c8], 0\n\tv_mfma_f32_32x32x16_bf16 %3, a[%c9:%c10], a[%c11:%c12], 0"
                 : "=v"(o0), "=v"(o1), "=v"(o2), "=v"(o3)
                 : "i"(NW), "i"(R), "i"(R + 3), "i"(R + 4), "i"(R + 7), "i"(R + 8), "i"(R + 11), "i"(R + 12), "i"(R + 15) : "memory");
  else
    asm volatile("s_waitcnt vmcnt(%c4)\n\tv_mfma_f32_32x32x16_bf16 %0, a[%c5:%c6], a[%c7:%c8], %0\n\tv_mfma_f32_32x32x16_bf16 %1, a[%c5:%c6], a[%c11:%c12], %1\n\t"
                 "v_mfma_f32_32x32x16_bf16 %2, a[%c9:%c10], a[%c7:%c8], %2\n\tv_mfma_f32_32x32x16_bf16 %3, a[%c9:%c10], a[%c11:%c12], %3"
                 : "+v"(o0), "+v"(o1), "+v"(o2), "+v"(o3)
                 : "i"(NW), "i"(R), "i"(R + 3), "i"(R + 4), "i"(R + 7), "i"(R + 8), "i"(R + 11), "i"(R + 12), "i"(R + 15) : "memory");
}
template <int R, bool FIRST> __device__ __forceinline__ void mfma_ba(f32x16& acc, const u32x4& A) {
  if constexpr (FIRST) asm volatile("v_mfma_f32_32x32x16_bf16 %0, %1, a[%c2:%c3], 0" : "=&v"(acc) : "v"(A), "i"(R), "i"(R + 3));
  else asm volatile("v_mfma_f32_32x32x16_bf16 %0, %1, a[%c2:%c3], %0" : "+v"(acc) : "v"(A), "i"(R), "i"(R + 3));
}
template <int R, int SO, int KO> __device__ __forceinline__ void a_read(u32 ax, u32 w0, u32 w1, u32 w2, u32 w3) {
  asm volatile("ds_read_b128 a[%c5:%c6], %0 offset:%c7\n\t"
               "ds_read_b64_tr_b16 a[%c5+4:%c5+5], %1 offset:%c8\n\tds_read_b64_tr_b16 a[%c5+6:%c5+7], %1 offset:%c9\n\t"
               "ds_read_b64_tr_b16 a[%c5+8:%c5+9], %2 offset:%c8\n\tds_read_b64_tr_b16 a[%c5+10:%c5+11], %2 offset:%c9\n\t"
               "ds_read_b64_tr_b16 a[%c5+12:%c5+13], %3 offset:%c8\n\tds_read_b64_tr_b16 a[%c5+14:%c5+15], %3 offset:%c9\n\t"
               "ds_read_b64_tr_b16 a[%c5+16:%c5+17], %4 offset:%c8\n\tds_read_b64_tr_b16 a[%c5+18:%c5+19], %4 offset:%c9"
               :: "v"(ax), "v"(w0), "v"(w1), "v"(w2), "v"(w3), "i"(R), "i"(R + 3), "i"(SO), "i"(SO + KO), "i"(SO + KO + 512) : "memory");
}
template <int R, int NW, bool FIRST> __device__ __forceinline__ void a_mma(f32x16& o0, f32x16& o1, f32x16& o2, f32x16& o3) {
  if constexpr (FIRST)
    asm volatile("s_waitcnt lgkmcnt(%c5)\n\tv_mfma_f32_32x32x16_bf16 %0, a[%c4+4:%c4+7], a[%c4:%c4+3], 0\n\tv_mfma_f32_32x32x16_bf16 %1, a[%c4+8:%c4+11], a[%c4:%c4+3], 0\n\t"
                 "v_mfma_f32_32x32x16_bf16 %2, a[%c4+12:%c4+15], a[%c4:%c4+3], 0\n\tv_mfma_f32_32x32x16_bf16 %3, a[%c4+16:%c4+19], a[%c4:%c4+3], 0"
                 : "=v"(o0), "=v"(o1), "=v"(o2), "=v"(o3) : "i"(R), "i"(NW) : "memory");
  else
    asm volatile("s_waitcnt lgkmcnt(%c5)\n\tv_mfma_f32_32x32x16_bf16 %0, a[%c4+4:%c4+7], a[%c4:%c4+3], %0\n\tv_mfma_f32_32x32x16_bf16 %1, a[%c4+8:%c4+11], a[%c4:%c4+3], %1\n\t"
                 "v_mfma_f32_32x32x16_bf16 %2, a[%c4+12:%c4+15], a[%c4:%c4+3], %2\n\tv_mfma_f32_32x32x16_bf16 %3, a[%c4+16:%c4+19], a[%c4:%c4+3], %3"
                 : "+v"(o0), "+v"(o1), "+v"(o2), "+v"(o3) : "i"(R), "i"(NW) : "memory");
}
template <int R, int OFF> __device__ __forceinline__ void p2_read(u32 ad) {
  asm volatile("ds_read_b64_tr_b16 a[%c1:%c1+1], %0 offset:%c2\n\tds_read_b64_tr_b16 a[%c1+2:%c1+3], %0 offset:%c3" :: "v"(ad), "i"(R), "i"(OFF), "i"(OFF + 384) : "memory");
}
template <int RA, int RB, int NW, bool FIRST> __device__ __forceinline__ void p2_mma(f32x16& acc) {
  if constexpr (FIRST) asm volatile("s_waitcnt lgkmcnt(%c3)\n\tv_mfma_f32_32x32x16_bf16 %0, a[%c1:%c1+3], a[%c2:%c2+3], 0" : "=v"(acc) : "i"(RA), "i"(RB), "i"(NW) : "memory");
  else asm volatile("s_waitcnt lgkmcnt(%c3)\n\tv_mfma_f32_32x32x16_bf16 %0, a[%c1:%c1+3], a[%c2:%c2+3], %0" : "+v"(acc) : "i"(RA), "i"(RB), "i"(NW) : "memory");
}
template <int R, int RN, int SO, int KO, bool FIRST> __device__ __forceinline__ void a_step(f32x16& o0, f32x16& o1, f32x16& o2, f32x16& o3, u32 ax, u32 w0, u32 w1, u32 w2, u32 w3) {
#define A_STEP_BODY(C0, C1, C2, C3) \
    "s_waitcnt lgkmcnt(9)\n\tv_mfma_f32_32x32x16_bf16 %0, a[%c9+4:%c9+7], a[%c9:%c9+3], " C0 "\n\t" \
    "ds_read_b128 a[%c10:%c10+3], %4 offset:%c11\n\tds_read_b64_tr_b16 a[%c10+4:%c10+5], %5 offset:%c12\n\tds_read_b64_tr_b16 a[%c10+6:%c10+7], %5 offset:%c13\n\t" \
    "v_mfma_f32_32x32x16_bf16 %1, a[%c9+8:%c9+11], a[%c9:%c9+3], " C1 "\n\t" \
    "ds_read_b64_tr_b16 a[%c10+8:%c10+9], %6 offset:%c12\n\tds_read_b64_tr_b16 a[%c10+10:%c10+11], %6 offset:%c13\n\t" \
    "v_mfma_f32_32x32x16_bf16 %2, a[%c9+12:%c9+15], a[%c9:%c9+3], " C2 "\n\t" \
    "ds_read_b64_tr_b16 a[%c10+12:%c10+13], %7 offset:%c12\n\tds_read_b64_tr_b16 a[%c10+14:%c10+15], %7 offset:%c13\n\t" \
    "v_mfma_f32_32x32x16_bf16 %3, a[%c9+16:%c9+19], a[%c9:%c9+3], " C3 "\n\t" \
    "ds_read_b64_tr_b16 a[%c10+16:%c10+17], %8 offset:%c12\n\tds_read_b64_tr_b16 a[%c10+18:%c10+19], %8 offset:%c13"
  if constexpr (FIRST)
    asm volatile(A_STEP_BODY("0", "0", "0", "0") : "=&v"(o0), "=&v"(o1), "=&v"(o2), "=&v"(o3)
                 : "v"(ax), "v"(w0), "v"(w1), "v"(w2), "v"(w3), "i"(R), "i"(RN), "i"(SO), "i"(SO + KO), "i"(SO + KO + 512) : "memory");
  else
    asm volatile(A_STEP_BODY("%0", "%1", "%2", "%3") : "+v"(o0), "+v"(o1), "+v"(o2), "+v"(o3)
                 : "v"(ax), "v"(w0), "v"(w1), "v"(w2), "v"(w3), "i"(R), "i"(RN), "i"(SO), "i"(SO + KO), "i"(SO + KO + 512) : "memory");
}
template <int RB_, bool FIRST> __device__ __forceinline__ void p2_mmv(f32x16& acc, const u32x4& A) {
  if constexpr (FIRST) asm volatile("v_mfma_f32_32x32x16_bf16 %0, %1, a[%c2:%c2+3], 0" : "=&v"(acc) : "v"(A), "i"(RB_) : "memory");
  else asm volatile("v_mfma_f32_32x32x16_bf16 %0, %1, a[%c2:%c2+3], %0" : "+v"(acc) : "v"(A), "i"(RB_) : "memory");
}
__device__ __forceinline__ void mfma_corr(f32x16& acc, const u32x4& A, const u32x4& B) { asm volatile("s_nop 1\n\tv_mfma_f32_32x32x16_bf16 %0, %1, %2, %0" : "+v"(acc) : "v"(A), "v"(B)); }
__device__ __forceinline__ void pin(f32x16& v) { asm volatile("" : "+v"(v)); }
#define NOP16(x) asm volatile("s_nop 7\n\ts_nop 7" : "+v"(x))
__device__ __forceinline__ float dpp_add(float v, const int ctrl, const int rm) {
  return v;
}
#define DPPADD(v, ctrl, rm) v += __builtin_bit_cast(float, __builtin_amdgcn_update_dpp(0, __builtin_bit_cast(int, v), ctrl, rm, 0xf, true))
__device__ __forceinline__ float wave_sum(float v) {
  DPPADD(v, 0x111, 0xf); DPPADD(v, 0x112, 0xf); DPPADD(v, 0x114, 0xf); DPPADD(v, 0x118, 0xf);
  DPPADD(v, 0x142, 0xa); DPPADD(v, 0x143, 0xc);
  return __builtin_bit_cast(float, __builtin_amdgcn_readlane(__builtin_bit_cast(int, v), 63));
}
__device__ __forceinline__ f32x2 cmul(f32x2 z, f32x2 w, f32x2 wc) {
  f32x2 zx = {z.x, z.x}, zy = {z.y, z.y};
  return __builtin_elementwise_fma(zy, wc, zx * w);
}
__device__ __forceinline__ f32x2 cexp(float a2, float brev, float t) {
  const float m = __builtin_amdgcn_exp2f(a2 * t), ph = __builtin_amdgcn_fractf(brev * t);
  return (f32x2){m * __builtin_amdgcn_cosf(ph), m * __builtin_amdgcn_sinf(ph)};
}
__device__ __forceinline__ float ffma1(float a, float b, float c) { float r; asm("v_fma_f32 %0, %1, %2, %3" : "=v"(r) : "v"(a), "v"(b), "v"(c)); return r; }
__device__ __forceinline__ float fadd1(float a, float b) { float r; asm("v_add_f32 %0, %1, %2" : "=v"(r) : "v"(a), "v"(b)); return r; }
__device__ __forceinline__ void gg1(float t0, float t1, float k0, float k1, float& p0, float& p1) {
  asm volatile("v_mul_f32 %0, %2, %2\n\tv_mul_f32 %1, %3, %3\n\tv_fma_f32 %0, %0, %5, %4\n\tv_fma_f32 %1, %1, %5, %4" : "=&v"(p0), "=&v"(p1) : "v"(t0), "v"(t1), "v"(k0), "v"(k1)); }
__device__ __forceinline__ void gg2(float t0, float t1, float& p0, float& p1) {
  asm volatile("v_mul_f32 %0, %0, %2\n\tv_mul_f32 %1, %1, %3\n\tv_exp_f32 %0, %0\n\tv_exp_f32 %1, %1" : "+v"(p0), "+v"(p1) : "v"(t0), "v"(t1)); }
__device__ __forceinline__ void gg3(float s2, float& p0, float& p1) {
  asm volatile("v_fma_f32 %0, %0, %2, %2\n\tv_fma_f32 %1, %1, %2, %2\n\tv_rcp_f32 %0, %0\n\tv_rcp_f32 %1, %1" : "+v"(p0), "+v"(p1) : "v"(s2)); }
__device__ __forceinline__ u32 gg4(float t0, float u0, float t1, float u1, float p0, float p1) {
  float a, b; u32 r;
  asm volatile("v_mul_f32 %0, %3, %4\n\tv_mul_f32 %1, %5, %6\n\tv_mul_f32 %0, %0, %7\n\tv_mul_f32 %1, %1, %8\n\tv_cvt_pk_bf16_f32 %2, %0, %1"
      : "=&v"(a), "=&v"(b), "=v"(r) : "v"(t0), "v"(u0), "v"(t1), "v"(u1), "v"(p0), "v"(p1));
  return r; }
__device__ __forceinline__ u32x4 geglu8(const f32x16& a, float k0, float k1, float s2) {
  u32x4 o;
#pragma unroll
  for (int i = 0; i < 4; ++i) {
    float p0, p1;
    gg1(a[2 * i], a[2 * i + 1], k0, k1, p0, p1); gg2(a[2 * i], a[2 * i + 1], p0, p1); gg3(s2, p0, p1);
    o[i] = gg4(a[2 * i], a[2 * i + 8], a[2 * i + 1], a[2 * i + 9], p0, p1);
  }
  return o;
}
__device__ __forceinline__ u32 ktab0(u32 m) {
  const u32 hi = m >> 5;
  const u32 r0 = hi == 0 ? 0u : (hi < 4 ? (hi == 1 ? 8u : 8u + 2u * hi) : 12u + hi);
  return r0 * 64u + (m & 31u) * 2u;
}

template <bool PK> __device__ __forceinline__ f32x2 cm(f32x2 z, f32x2 w) {
  if constexpr (PK) { const f32x2 wc = {-w.y, w.x}; return cmul(z, w, wc); }
  else {
    float re, im;
    asm("v_mul_f32 %0, %2, %4\n\tv_mul_f32 %1, %2, %5\n\tv_fma_f32 %0, -%3, %5, %0\n\tv_fma_f32 %1, %3, %4, %1" : "=&v"(re), "=&v"(im) : "v"(z.x), "v"(z.y), "v"(w.x), "v"(w.y));
    return (f32x2){re, im};
  }
}
template <bool PK> __device__ __forceinline__ void kgen(const u32 (&pr)[6], u32 ldt, u32 so, u32 po, int lane) {
  const int i16 = lane & 15, g4 = lane >> 4;
  const float dt = rbf(__builtin_amdgcn_exp2f(bf2f(ldt) * 1.4426950408889634f));
  const float a2 = dt * bf2f(pr[0]) * 1.4426950408889634f, br = dt * bf2f(pr[1]) * 0.15915494309189535f;
  const f32x2 c = cm<PK>((f32x2){bf2f(pr[4]), -bf2f(pr[5])}, (f32x2){dt * bf2f(pr[2]), -dt * bf2f(pr[3])});
  const f32x2 e1 = cexp(a2, -br, 1.f), e256 = cexp(a2, -br, 256.f), e8 = cexp(a2, br, 8.f), e64 = cexp(a2, br, 64.f), e128 = cexp(a2, br, 128.f);
  const f32x2 e2 = cm<PK>(e1, e1), e4 = cm<PK>(e2, e2);
  const u32 rp = so + i16 * KG_STR + g4 * 16;
  const u32 rp5 = so + (i16 < 5 ? i16 : 4) * KG_STR + g4 * 16;
#define KW(row, z) dsw_tid<(row) * KG_STR>(pk_bf16((z).x, (z).y))
  u32x4 fa0[4], fa1[4], fb[4];
  setm0(so);
  {
    f32x2 z = c, y = cm<PK>(c, e256);
    const f32x2 a512 = cm<PK>(y, e256), a768 = cm<PK>(a512, e256);
    f32x2 u = cm<PK>(a768, e256);
    static_for<0, 8>([&](auto ic) AINL {
      constexpr int k = ic.value;
      if constexpr (k > 0) z = cm<PK>(z, e1);
      KW(k, z);
      if constexpr (k > 0 && k < 4) y = cm<PK>(y, e2);
      if constexpr (k == 4) y = a512;
      if constexpr (k == 5) y = cm<PK>(a512, e4);
      if constexpr (k == 6) y = a768;
      if constexpr (k == 7) y = cm<PK>(a768, e4);
      KW(8 + k, y);
    });
    kg_read(fa0, rp);
    static_for<0, 5>([&](auto ic) AINL { KW(ic.value, u); if constexpr (ic.value < 4) u = cm<PK>(u, e256); });
    kg_read(fa1, rp5);
  }
  f32x4 kacc[2][2];
  static_for<0, 2>([&](auto hc) AINL {
    constexpr int hb = hc.value;
    f32x2 z = hb ? e128 : (f32x2){1.f, 0.f}, y = hb ? cm<PK>(e128, e64) : e64;
    static_for<0, 8>([&](auto ic) AINL {
      constexpr int k = ic.value;
      if constexpr (k > 0) { z = cm<PK>(z, e8); y = cm<PK>(y, e8); }
      KW(k, z); KW(8 + k, y);
    });
    if constexpr (hb == 1) {
      asm volatile("s_waitcnt lgkmcnt(15)" : "+v"(fa0[0]), "+v"(fa0[1]), "+v"(fa0[2]), "+v"(fa0[3]), "+v"(fa1[0]), "+v"(fa1[1]), "+v"(fa1[2]), "+v"(fa1[3]),
                   "+v"(fb[0]), "+v"(fb[1]), "+v"(fb[2]), "+v"(fb[3]) :: "memory");
      MFMA16_0(kacc[0][0], fa0[0], fb[0]); MFMA16_0(kacc[1][0], fa1[0], fb[0]);
#pragma unroll
      for (int ks = 1; ks < 4; ++ks) { MFMA16(kacc[0][0], fa0[ks], fb[ks]); MFMA16(kacc[1][0], fa1[ks], fb[ks]); }
    }
    kg_read(fb, rp);
  });
  setm0(po);
  asm volatile("s_waitcnt lgkmcnt(0)" : "+v"(fb[0]), "+v"(fb[1]), "+v"(fb[2]), "+v"(fb[3]) :: "memory");
  MFMA16_0(kacc[0][1], fa0[0], fb[0]); MFMA16_0(kacc[1][1], fa1[0], fb[0]);
#pragma unroll
  for (int ks = 1; ks < 4; ++ks) { MFMA16(kacc[0][1], fa0[ks], fb[ks]); MFMA16(kacc[1][1], fa1[ks], fb[ks]); }
  asm volatile("ds_write_addtid_b32 %0\n\tds_write_addtid_b32 %1 offset:256\n\tds_write_addtid_b32 %2 offset:512\n\tds_write_addtid_b32 %3 offset:768\n\t"
               "s_mov_b32 exec_hi, 0\n\t"
               "ds_write_addtid_b32 %4 offset:2048\n\tds_write_addtid_b32 %5 offset:2176\n\tds_write_addtid_b32 %6 offset:2304\n\tds_write_addtid_b32 %7 offset:2432\n\t"
               "s_mov_b32 exec_hi, -1\n\ts_nop 3"
               :: "v"(kacc[0][0][0]), "v"(kacc[0][0][1]), "v"(kacc[0][0][2]), "v"(kacc[0][0][3]), "v"(kacc[1][0][0]), "v"(kacc[1][0][1]), "v"(kacc[1][0][2]), "v"(kacc[1][0][3]) : "memory");
  asm volatile("ds_write_addtid_b32 %0 offset:1024\n\tds_write_addtid_b32 %1 offset:1280\n\tds_write_addtid_b32 %2 offset:1536\n\tds_write_addtid_b32 %3 offset:1792\n\t"
               "s_mov_b32 exec_hi, 0\n\t"
               "ds_write_addtid_b32 %4 offset:2560\n\tds_write_addtid_b32 %5 offset:2688\n\tds_write_addtid_b32 %6 offset:2816\n\tds_write_addtid_b32 %7 offset:2944\n\t"
               "s_mov_b32 exec_hi, -1"
               :: "v"(kacc[0][1][0]), "v"(kacc[0][1][1]), "v"(kacc[0][1][2]), "v"(kacc[0][1][3]), "v"(kacc[1][1][0]), "v"(kacc[1][1][1]), "v"(kacc[1][1][2]), "v"(kacc[1][1][3]) : "memory");
}
__device__ __forceinline__ void bsync(LAS char* cnt, int lane) {
  asm volatile("s_waitcnt lgkmcnt(0)" ::: "memory");
  if (lane == 0) __hip_atomic_fetch_add((LAS u32*)cnt, 1u, __ATOMIC_RELAXED, __HIP_MEMORY_SCOPE_WORKGROUP);
#pragma nounroll
  for (u32 s = 0; s < (SPIN_CAP << 4); ++s) { if (*(LAS volatile u32*)cnt >= 4u) break; }
  asm volatile("" ::: "memory");
}

struct Params {
  const u16 *x, *Are, *Aim, *Bre, *Bim, *Cre, *Cim, *logdt, *dskip, *wg, *wu, *wd;
  u16* out; char* ws;
};

#define STAMP(i) do {} while (0)
#define STAMP_OUT() do {} while (0)

template <bool grpB> __device__ __forceinline__ void run(const Params& P, LAS char* lds, const int w) {
  const u32 lbase = (u32)(size_t)lds;
  const int tid = threadIdx.x, lane = tid & 63;
  const int q = w & 3;
  const int b = blockIdx.x, xcd = b & 7, rr = b >> 3;
  const int mt = (xcd >> 2) * 8 + (rr >> 2), nt = (xcd & 3) * 4 + (rr & 3);
  const int mt2 = xcd * 4 + (rr >> 3), ct = rr & 7;
  const int c0 = nt >> 1 < 7 ? nt >> 1 : 0;
  const bool have2 = b + 256 < D;
  const int a0 = nt & 3;
  const int t31 = lane & 31, hh = lane >> 5, g4 = lane >> 4, i16 = lane & 15;
  const int tb = tid & 255;
  asm volatile("" ::: "a0", "a127");
  u32 cdum = 0;
  if constexpr (!grpB) {
    u64 pc;
    asm volatile("s_getpc_b64 %0" : "=s"(pc));
    u32 ln = (rr * 4 + q) * 4 + (lane & 3); ln = ln < CODE_LINES ? ln : CODE_LINES - 1;
    asm volatile("global_load_dword %0, %1, %2" : "=v"(cdum) : "v"(ln * 128), "s"(pc & ~255ull) : "memory");
  }
  const u32 xfa = (u32)((32 * q + t31) * 128 + ((hh ^ ((t31 >> 1) & 7)) * 16));
  u32 xa[2][4];
#pragma unroll
  for (int k = 0; k < 4; ++k) { xa[0][k] = lbase + RB + (xfa ^ (k * 32)); PINV(xa[0][k]); if constexpr (!grpB) { xa[1][k] = xa[0][k] + 2 * RS; PINV(xa[1][k]); } }
  u32 vx[4], vw[2], vu[2], vb[3], ix[9], xta[4], aw[2][4];
#pragma unroll
  for (int p = 0; p < 4; ++p) { vx[p] = (u32)(8 * p + (lane >> 3)) * 896 + (((lane & 7) ^ g4 ^ (4 * (p & 1))) * 16) + 4096 - 1024 * p; PINV(vx[p]); }
#pragma unroll
  for (int p = 0; p < 2; ++p) {
    vw[p] = (u32)(8 * p + (lane >> 3)) * 3584 + ((((lane & 7) >> 1) ^ (g4 & 1)) * 32) + (lane & 1) * 16 + 4096 - 1024 * p; PINV(vw[p]);
    vu[p] = vw[p] ^ 64; PINV(vu[p]); }
  if constexpr (grpB) {
#pragma unroll
    for (int p = 0; p < 4; ++p) {
      const int t = 64 * (q >> 1) + 32 * (p >> 1) + 8 * g4 + 4 * (p & 1) + (i16 >> 2);
      const int c8 = 4 * (nt & 1) + 2 * (q & 1) + ((i16 & 3) >> 1);
      xta[p] = (u32)(RB + RS + t * 128 + ((c8 ^ ((t >> 1) & 7)) * 16) + (i16 & 1) * 8); PINV(xta[p]);
    }
  } else {
    const u32 wfa = (u32)(16384 + (g4 & 1) * 8192 + (8 * (g4 >> 1) + (i16 >> 2)) * 128 + ((((i16 >> 3) & 1) + 2 * (g4 & 1)) * 32) + 8 * (i16 & 3));
#pragma unroll
    for (int k = 0; k < 4; ++k) { aw[0][k] = lbase + RB + (wfa ^ (k * 32)); aw[1][k] = aw[0][k] + 2 * RS; PINV(aw[0][k]); PINV(aw[1][k]); }
  }
  u32 vtw = 0;
  if constexpr (!grpB) { const int l = lane < 49 ? lane : 48, r = (l * 37) >> 8; vtw = (u32)(14 * rr + 7 * (q >> 1) + r) * 3584 + (xcd & 3) * 896 + (l - 7 * r) * 128; PINV(vtw); }
  u32 vcs = (u32)(lane >> 3) * 3584 + ((lane & 7) < 7 ? (lane & 7) : 6) * 4; PINV(vcs);
  const int kch = (grpB && have2) ? b + 256 : b;
  u32 vpr = (u32)(kch * 256 + q * 64 + lane) * 2; PINV(vpr);
  u32 vst = (u32)(lane < 56 ? lane : 55) * 16; PINV(vst);
  if (tid < 2) { *(LAS u32x4*)(lds + L_FLG + tid * 16) = (u32x4){0, 0, 0, 0}; }
  char* ws = P.ws;
  STAMP(0);

  u32 ep, wdum = 0;
  if constexpr (!grpB) gld4(wdum, vtw, (q & 1) ? P.wu : P.wg);
  gld4_sc1(ep, 0, ws + WS_EPOCH);
  u32x4 xs; u32 cv[7], pr[6], wdv[16], ldt, dsk0, dsk1;
  gld16(xs, vst, (const char*)P.x + (size_t)(128 * mt + 8 * nt + w) * 896);
  {
    const char* cb = (const char*)(mt < 8 ? P.wg : P.wu) + (size_t)(56 * w) * 3584 + 224 * nt + 28 * (mt & 7);
#pragma unroll
    for (int i = 0; i < 7; ++i) gld4(cv[i], vcs, cb + i * 8 * 3584);
  }
  const char* xg = (const char*)P.x + (size_t)(128 * mt + 32 * q) * 896 - 4096;
  const char* wgp = (const char*)P.wg + (size_t)(16 * q) * 3584 + 224 * nt + 32 * a0 - 4096;
  const char* wup = (const char*)P.wu + (size_t)(16 * q) * 3584 + 224 * nt + 32 * a0 - 4096;
  auto dmaX = [&](int st) AINL {
    int c = st + c0; c = c >= 7 ? c - 7 : c;
    const char* p_ = xg + c * 128;
    setm0(lbase + RB + ((st + 1) & 3) * RS + q * 4096);
    dma<0>(vx[0], p_); dma<1024>(vx[1], p_); dma<2048>(vx[2], p_); dma<3072>(vx[3], p_);
  };
  auto dmaG = [&](int st) AINL {
    int c = st + c0; c = c >= 7 ? c - 7 : c;
    const char* p_ = wgp + (u32)c * (64u * 3584u);
    setm0(lbase + RB + ((st + 1) & 3) * RS + 16384 + q * 2048);
    dma<0>(vw[0], p_); dma<1024>(vw[1], p_);
  };
  auto dmaU = [&](int st) AINL {
    int c = st + c0; c = c >= 7 ? c - 7 : c;
    const char* p_ = wup + (u32)c * (64u * 3584u);
    setm0(lbase + RB + ((st + 1) & 3) * RS + 16384 + 8192 + q * 2048);
    dma<0>(vu[0], p_); dma<1024>(vu[1], p_);
  };
  auto ld_params = [&]() AINL {
    gld2(pr[0], vpr, P.Are); gld2(pr[1], vpr, P.Aim); gld2(pr[2], vpr, P.Bre);
    gld2(pr[3], vpr, P.Bim); gld2(pr[4], vpr, P.Cre); gld2(pr[5], vpr, P.Cim);
    gld2(ldt, kch * 2, P.logdt);
  };
  constexpr int SS = grpB ? 0 : 1;
  dmaX(SS); dmaG(SS); dmaU(SS);
  WAITV(15);
  PINV(xs); PINV(ep); PINV(cdum); PINV(wdum);
  const u32 T = __builtin_amdgcn_readfirstlane(ep) + 1u;
  STAMP(8);
  {
    const bf16x2 one2 = __builtin_bit_cast(bf16x2, 0x3f803f80u);
    float s1 = 0.f, s2 = 0.f;
#pragma unroll
    for (int i = 0; i < 4; ++i) {
      const bf16x2 v = __builtin_bit_cast(bf16x2, lane < 56 ? xs[i] : 0u);
      s1 = __builtin_amdgcn_fdot2_f32_bf16(v, one2, s1, false); s2 = __builtin_amdgcn_fdot2_f32_bf16(v, v, s2, false);
    }
    auto r = __builtin_amdgcn_permlane32_swap(__float_as_uint(s1), __float_as_uint(s2), false, false);
    float s = __uint_as_float(r[0]) + __uint_as_float(r[1]);
    DPPADD(s, 0x111, 0xf); DPPADD(s, 0x112, 0xf); DPPADD(s, 0x114, 0xf); DPPADD(s, 0x118, 0xf); DPPADD(s, 0x142, 0xa);
    const float mean = __builtin_bit_cast(float, __builtin_amdgcn_readlane(__builtin_bit_cast(int, s), 31)) * (1.f / 448.f);
    const float msq = __builtin_bit_cast(float, __builtin_amdgcn_readlane(__builtin_bit_cast(int, s), 63)) * (1.f / 448.f);
    const float var = rbf(fmaxf(fmaf(-mean, mean, msq), 0.f));
    const float sd = __builtin_amdgcn_sqrtf(rbf(var + 1e-5f));
    const u32x2 g = {pk_bf16(mean, sd), T};
    if (lane == 0) gst8_sc1<0>(0, g, ws + WS_ST + (size_t)(128 * mt + 8 * nt + w) * 8);
  }
  dmaX(SS + 2);
  WAITV(13);
  {
    PINV(cv[0]); PINV(cv[1]); PINV(cv[2]); PINV(cv[3]); PINV(cv[4]); PINV(cv[5]); PINV(cv[6]);
    const bf16x2 lo1 = __builtin_bit_cast(bf16x2, 0x3f803f80u), hi1 = __builtin_bit_cast(bf16x2, 0x3f800000u);
    float s0 = 0.f, s1 = 0.f;
#pragma unroll
    for (int i = 0; i < 7; ++i) {
      s0 = __builtin_amdgcn_fdot2_f32_bf16(__builtin_bit_cast(bf16x2, cv[i]), lo1, s0, false);
      s1 = __builtin_amdgcn_fdot2_f32_bf16(__builtin_bit_cast(bf16x2, cv[i]), hi1, s1, false);
    }
    s0 -= s1;
    auto r = __builtin_amdgcn_permlane32_swap(__float_as_uint(s0), __float_as_uint(s1), false, false);
    float s = __uint_as_float(r[0]) + __uint_as_float(r[1]);
    auto r2 = __builtin_amdgcn_permlane16_swap(__float_as_uint(s), __float_as_uint(s), false, false);
    s = __uint_as_float(r2[0]) + __uint_as_float(r2[1]);
    DPPADD(s, 0x128, 0xf);
    *(LAS float*)(lds + L_CSP + (w * 16 + hh * 8 + (lane & 7)) * 4) = s;
  }
  dmaG(SS + 2); dmaU(SS + 2);
  if constexpr (grpB) {
    const int ub = b < 224 ? b : 0;
    const char* wb_ = (const char*)P.wd + (size_t)(16 * (ub >> 1) + 4 * (ub & 1)) * 896 + 224 * q;
    const u32 vo = vst >> 3;
    static_for<0, 16>([&](auto ic) AINL { constexpr int j = ic.value; gld2o<(j & 3) * 896 + (j >> 3) * 112>(wdv[j], vo, wb_ + 8 * ((j >> 2) & 1) * 896); });
  }
  STAMP(1);

  if constexpr (!grpB) {
    f32x16 acc[4];
    BARRIER();
    STAMP(10);
    __builtin_amdgcn_s_setprio(1);
#define A_AD(n) xa[((((n) >> 2) + 1) & 3) >> 1][(n) & 3], aw[((((n) >> 2) + 1) & 3) >> 1][0], aw[((((n) >> 2) + 1) & 3) >> 1][1], aw[((((n) >> 2) + 1) & 3) >> 1][2], aw[((((n) >> 2) + 1) & 3) >> 1][3]
#define A_SO(n) (((((n) >> 2) + 1) & 1) * RS)
    a_read<0, A_SO(0), 0>(A_AD(0)); a_read<20, A_SO(1), 2048>(A_AD(1));
    static_for<0, 28>([&](auto ic) AINL {
      constexpr int n = ic.value;
      if constexpr (n == 2) WAITV(9);
      if constexpr (n == 10) WAITV(0);
      if constexpr (n == 20) ld_params();
      if constexpr ((n & 3) == 2) { STAMP(16 + (n >> 2)); BARRIER(); STAMP(24 + (n >> 2)); }
      if constexpr (n + 2 < 28) a_step<20 * (n % 3), 20 * ((n + 2) % 3), A_SO(n + 2), ((n + 2) & 3) * 2048, n == 0>(acc[0], acc[1], acc[2], acc[3], A_AD(n + 2));
      else a_mma<20 * (n % 3), 0, false>(acc[0], acc[1], acc[2], acc[3]);
    });
    __builtin_amdgcn_s_setprio(0);
    STAMP(2);
    u32 fwv;
    const int cs = q >> 1, cp = q & 1;
    const int ch = (cs && have2) ? b + 256 : b;
    u32 fxv;
    const u32 stt = *(LAS const u32*)(lds + L_ST + (32 * q + t31) * 4);
    const float sd = bfhi(stt), rs = __builtin_amdgcn_rcpf(sd);
    const float k0 = -2.3022081986f * rs, k1 = k0 * 0.044715f * rs * rs, s2 = sd * sd;
    const u32 nm = hh ? 0u : (((stt ^ 0x8000u) & 0xffffu) * 0x10001u);
    const u32x4 mb = {nm, 0, 0, 0};
#pragma unroll
    for (int j = 0; j < 4; ++j) {
      const float c = *(LAS const float*)(lds + L_CS + ((g4 & 1) * 112 + 16 * (a0 + j) + i16) * 4);
      const float chi = rbf(c);
      const u32x4 ca = {pk_bf16(chi, c - chi), 0, 0, 0};
      mfma_corr(acc[j], ca, mb);
    }
    STAMP(12);
    BARRIER();
    STAMP(13);
    const char* wb = ws + WS_WD + ((size_t)ct * 112 + 28 * q) * 1792 + 2688;
    const char* hb0 = ws + WS_HF + ((size_t)(2 * mt2) * 112 + 28 * q) * 1024;
    const char* hb1 = hb0 + 112 * 1024;
    const u32 hoff = lane * 16;
    const u32 woff0 = hh * 896 + t31 * 16;
    const u32 woff1 = hh * 896 + (t31 < 24 ? 32 + t31 : 55) * 16;
#define KTILE(s) ((s) < 16 ? 8 * ((s) >> 2) + ((s) & 3) : 7 * (((s) - 16) / 3) + ((((s) - 16) % 3) < (((s) - 16) / 3) ? (((s) - 16) % 3) : (((s) - 16) % 3) + 4))
#define KB(s) (KTILE(s) & ~3)
#define KO(s) (KTILE(s) & 3)
#define LDW(s) do { gld16a_sc1<16 * ((s) & 7), KO(s) * 1792 - 2688>(woff0, wb + KB(s) * 1792); gld16a_sc1<16 * ((s) & 7) + 8, KO(s) * 1792 - 2688>(woff1, wb + KB(s) * 1792); } while (0)
#define LDH(s) do { gld16a_sc1<16 * ((s) & 7) + 4, KO(s) * 1024>(hoff, hb0 + KB(s) * 1024); gld16a_sc1<16 * ((s) & 7) + 12, KO(s) * 1024>(hoff, hb1 + KB(s) * 1024); } while (0)
    char* hdst = ws + WS_HF + ((size_t)(4 * mt + q) * 112 + 7 * nt + a0) * 1024;
    NOP16(acc[0]);
    gst16o_sc1<0>(hoff, geglu8(acc[0], k0, k1, s2), hdst);
    pin(acc[1]); gst16o_sc1<1024>(hoff, geglu8(acc[1], k0, k1, s2), hdst);
    pin(acc[2]); gst16o_sc1<2048>(hoff, geglu8(acc[2], k0, k1, s2), hdst);
    gld4_sc1(fwv, (u32)((ct >> 1) * 224 + 56 * q + (lane < 56 ? lane : 0)) * 4, ws + WS_FW);
    gld4_sc1(fxv, (u32)((ch >> 5) * 64 + lane) * 4, ws + WS_FX);
    pin(acc[3]); gst16o_sc1<3072>(hoff, geglu8(acc[3], k0, k1, s2), hdst);
    STAMP(14);
    WAITV(1); PINV(fwv); PINV(fxv);
    if (!__all(fxv == T)) poll_flags((u32)((ch >> 5) * 64 + lane) * 4, ws + WS_FX, T);
    {
      const char* urb = ws + WS_XT + (size_t)ch * (XT_ROW * 2) + (cp ? 124 : 128) - 4096;
      setm0(lbase + L_U + q * U_SZ + 15 * 144);
      u32 s = lane;   dma_sc1<0>((s - ((s * 57) >> 9)) * 16 + 4096, urb);
      s = lane + 64;  dma_sc1<1024>((s - ((s * 57) >> 9)) * 16 + 3072, urb);
      s = lane + 128; dma_sc1<2048>((s - ((s * 57) >> 9)) * 16 + 2048, urb);
      s = lane + 192; dma_sc1<3072>((s - ((s * 57) >> 9)) * 16 + 1024, urb);
      setm0(lbase + L_U + q * U_SZ + 15 * 144 + 4096);
      s = lane + 256; dma_sc1<0>((s - ((s * 57) >> 9)) * 16 + 4096, urb);
      const u32x4 z = {0, 0, 0, 0};
      LAS char* ub = lds + L_U + q * U_SZ;
      *(LAS u32x4*)(ub + lane * 16) = z; *(LAS u32x4*)(ub + 1024 + lane * 16) = z;
      if (lane < 7) *(LAS u32x4*)(ub + 2048 + lane * 16) = z;
    }
    STAMP(15);
    WAITV(5);
    if (lane == 0) gst4_sc1(0, T, ws + WS_FH + ((4 * mt + q) * 32 + nt) * 4);
    if (!__all(fwv == T)) poll_flags((u32)((ct >> 1) * 224 + 56 * q + (lane < 56 ? lane : 0)) * 4, ws + WS_FW, T);
    static_for<0, 8>([&](auto ic) AINL { LDW(ic.value); });
    STAMP(9);
    PINV(pr[0]); PINV(pr[1]); PINV(pr[2]); PINV(pr[3]); PINV(pr[4]); PINV(pr[5]); PINV(ldt);
    kgen<false>(pr, ldt, lbase + L_KP + (2 * q) * KP_SZ, lbase + L_KP + (2 * q) * KP_SZ, lane);
    WAITV(16);
    STAMP(3);
    BARRIER();
    STAMP(4);
    const u32 fho = (u32)((2 * mt2 + ((lane >> 2) & 1)) * 32 + 4 * q + (lane & 3)) * 4;
    poll_flags(fho, ws + WS_FH, T);
    STAMP(5);
    static_for<0, 8>([&](auto ic) AINL { LDH(ic.value); });
    f32x16 o[4];
    u32x4 xv;
    u32 fbv;
    const u32 xo = (u32)(tid / 7) * 896 + (tid % 7) * 16;
    static_for<0, 28>([&](auto ic) AINL {
      constexpr int s = ic.value;
      constexpr int nw = s < 8 ? 14 + 2 * s : (s <= 19 ? 28 : 4 * (27 - s));
      constexpr int R = 16 * (s & 7);
      g2_step<nw, R, s == 0>(o[0], o[1], o[2], o[3]);
      if constexpr (s == 5) gld4_sc1(fbv, fho + 64, ws + WS_FH);
      if constexpr (s == 8) { WAITV(12); PINV(fbv); if (!__all(fbv == T)) poll_flags(fho + 64, ws + WS_FH, T); STAMP(11); }
      if constexpr (s + 8 < 28) { LDW(s + 8); LDH(s + 8); }
    });
    STAMP(6);
    gld16(xv, xo, (const char*)P.x + (size_t)mt2 * 64 * 896 + ct * 112);
    NOP16(o[3]);
#pragma unroll
    for (int cb = 0; cb < 2; ++cb)
#pragma unroll
      for (int tt = 0; tt < 2; ++tt) {
        pin(o[cb * 2 + tt]);
        LAS char* pp = lds + L_PART + q * PART + (tt * 32 + t31) * PSTR + (cb * 32 + 4 * hh) * 4;
#pragma unroll
        for (int g = 0; g < 4; ++g) {
          if (cb == 1 && g == 3) continue;
          *(LAS f32x4*)(pp + g * 32) = (f32x4){o[cb * 2 + tt][4 * g], o[cb * 2 + tt][4 * g + 1], o[cb * 2 + tt][4 * g + 2], o[cb * 2 + tt][4 * g + 3]};
        }
      }
    BARRIER();
    WAITV(0);
    PINV(xv);
    {
      LAS const char* pp = lds + (tid / 7) * PSTR + (tid % 7) * 32;
      f32x4 s0 = *(LAS const f32x4*)(pp + L_BASE), s1 = *(LAS const f32x4*)(pp + L_BASE + 16);
#pragma unroll
      for (int p = 0; p < 4; ++p) { s0 += *(LAS const f32x4*)(pp + L_PART + p * PART); s1 += *(LAS const f32x4*)(pp + L_PART + p * PART + 16); }
      u32x4 ov;
      ov.x = pk_bf16(s0[0] + bf2f(xv.x), s0[1] + bfhi(xv.x)); ov.y = pk_bf16(s0[2] + bf2f(xv.y), s0[3] + bfhi(xv.y));
      ov.z = pk_bf16(s1[0] + bf2f(xv.z), s1[1] + bfhi(xv.z)); ov.w = pk_bf16(s1[2] + bf2f(xv.w), s1[3] + bfhi(xv.w));
      *(u32x4*)((char*)P.out + (size_t)mt2 * 64 * 896 + ct * 112 + xo) = ov;
    }
    STAMP(7);
    STAMP_OUT();
  } else {
#define B_XREAD(st) asm volatile("ds_read_b128 a[%c4:%c4+3], %0 offset:%c5\n\tds_read_b128 a[%c4+4:%c4+7], %1 offset:%c5\n\t" \
      "ds_read_b128 a[%c4+8:%c4+11], %2 offset:%c5\n\tds_read_b128 a[%c4+12:%c4+15], %3 offset:%c5" \
      :: "v"(xa[(((st) + 1) >> 1) & 1][0]), "v"(xa[(((st) + 1) >> 1) & 1][1]), "v"(xa[(((st) + 1) >> 1) & 1][2]), "v"(xa[(((st) + 1) >> 1) & 1][3]), \
         "i"(16 * (st)), "i"((((st) + 1) & 1) * RS) : "memory")
    const char* wbs = (const char*)((q & 1) ? P.wu : P.wg) + 224 * nt - 4096;
    auto wb_dma = [&](int u, u32 dst) AINL {
      int us = u + 2 * c0; us = us >= 14 ? us - 14 : us;
      const char* s_ = wbs + (size_t)us * (32 * 3584);
      setm0(dst); dma<0>(vb[0], s_); dma<1024>(vb[1], s_); dma<2048>(vb[2], s_);
    };
    const u32 wbo = lbase + RB + (q & 1) * WB_UP;
    const int z2 = q >> 1;
    WAITV(25);
    __builtin_amdgcn_s_setprio(2);
    BARRIER();
    STAMP(10);
    B_XREAD(0);
    u32x2 xt[4];
#pragma unroll
    for (int m = 0; m < 4; ++m) xt[m] = trread(lds + xta[m]);
    if (w == 4) {
      float s = 0.f;
#pragma unroll
      for (int i = 0; i < 8; ++i) s += *(LAS const float*)(lds + L_CSP + (i * 16 + i16) * 4);
      const u32x2 g = {__float_as_uint(s), T};
      if (lane < 16 && (lane & 7) < 7)
        gst8_sc1<0>((u32)(2 * (lane & 7) + (lane >> 3)) * 8, g, ws + WS_CS + (size_t)((mt >> 3) * 1792 + 112 * nt + 14 * (mt & 7)) * 8);
    }
#pragma unroll
    for (int k = 0; k < 4; ++k) xa[1][k] = xa[0][k] + 2 * RS;
#pragma unroll
    for (int v = 0; v < 3; ++v) { const u32 s = 64 * v + lane, r = (s * 171) >> 10; const u32 c = s - 6 * r; vb[v] = r * 3584 + ((int)(c >> 1) < a0 ? c : c + 8) * 16 + 4096 - 1024 * v; }
    STAMP(16);
    BARRIER();
    STAMP(11);
    dmaX(4); dmaG(4); dmaU(4);
    B_XREAD(1);
    WAITV(8);
#pragma unroll
    for (int j = 0; j < 16; ++j) PINV(wdv[j]);
    if (lane < 56) {
      char* dst = b < 224 ? ws + WS_WD + ((size_t)(2 * q) * 112 * 2 + b) * 896 : ws + WS_DUMMY + (size_t)q * (256 << 10);
      gst16o_sc1<0>(vst, (u32x4){wdv[0] | (wdv[1] << 16), wdv[2] | (wdv[3] << 16), wdv[4] | (wdv[5] << 16), wdv[6] | (wdv[7] << 16)}, dst);
      gst16o_sc1<0>(vst, (u32x4){wdv[8] | (wdv[9] << 16), wdv[10] | (wdv[11] << 16), wdv[12] | (wdv[13] << 16), wdv[14] | (wdv[15] << 16)}, dst + 112 * 1792);
    }
    u32x4 stg;
    if (w == 5) gld16_sc1(stg, lane * 16, ws + WS_ST + (size_t)mt * 1024);
    STAMP(17);
    BARRIER();
    STAMP(12);
    dmaX(5); dmaG(5); dmaU(5);
    B_XREAD(2);
    STAMP(18);
    BARRIER();
    STAMP(13);
    u32x4 cg0, cg1;
    if (w == 6) {
      gld16_sc1(cg0, vst, ws + WS_CS + (size_t)(112 * nt) * 8); gld16_sc1(cg1, vst, ws + WS_CS + (size_t)(1792 + 112 * nt) * 8);
    }
    dmaX(6); dmaG(6); dmaU(6);
    B_XREAD(3);
    WAITV(16);
    if (lane == 0 && b < 224) gst4_sc1(0, T, ws + WS_FW + (q * 224 + b) * 4);
    if (w == 5) {
      PINV(stg);
#pragma nounroll
      for (u32 s = 0; s < SPIN_CAP; ++s) {
        if (__all(stg.y == T && stg.w == T)) break;
        __builtin_amdgcn_s_sleep(1);
        asm volatile("global_load_dwordx4 %0, %1, %2 sc1\n\ts_waitcnt vmcnt(0)" : "=v"(stg) : "v"(lane * 16), "s"(ws + WS_ST + (size_t)mt * 1024) : "memory");
      }
      *(LAS u32x2*)(lds + L_ST + lane * 8) = (u32x2){stg.x, stg.z};
      const float r0 = __builtin_amdgcn_rcpf(bfhi(stg.x)), r1 = __builtin_amdgcn_rcpf(bfhi(stg.z));
      *(LAS f32x4*)(lds + L_ST2 + lane * 16) = (f32x4){r0, -bf2f(stg.x) * r0, r1, -bf2f(stg.z) * r1};
    }
    STAMP(19);
    BARRIER();
    STAMP(14);
    wb_dma(z2, wbo + z2 * 3072);
    B_XREAD(4);
    {
      u32 o[8];
      LAS const char* sp = lds + L_ST2 + (64 * (q >> 1) + 8 * g4) * 8;
#pragma unroll
      for (int m = 0; m < 4; ++m) {
        const f32x4 sa = *(LAS const f32x4*)(sp + (m >> 1) * 256 + (m & 1) * 32), sb = *(LAS const f32x4*)(sp + (m >> 1) * 256 + (m & 1) * 32 + 16);
        o[2 * m] = pk_bf16(ffma1(bf2f(xt[m].x), sa[0], sa[1]), ffma1(bfhi(xt[m].x), sa[2], sa[3]));
        o[2 * m + 1] = pk_bf16(ffma1(bf2f(xt[m].y), sb[0], sb[1]), ffma1(bfhi(xt[m].y), sb[2], sb[3]));
      }
      wb_dma(z2 + 2, wbo + (z2 + 2) * 3072);
      u32x4 s1, s2;
#pragma unroll
      for (int k = 0; k < 4; ++k) {
        const u32 x = i16 < 8 ? o[4 + k] : o[k];
        const u32 y = (u32)__builtin_amdgcn_update_dpp(0, (int)x, 0x128, 0xf, 0xf, true);
        s1[k] = i16 < 8 ? o[k] : y; s2[k] = i16 < 8 ? y : o[4 + k];
      }
      char* dst = nt < 14 ? ws + WS_XT + (size_t)(32 * nt + 16 * (q & 1)) * (XT_ROW * 2) + 128 + (128 * mt + 64 * (q >> 1)) * 2
                          : ws + WS_DUMMY + (128 * mt + 64 * (q >> 1)) * 2;
      const u32 vo = (u32)(i16 & 7) * (XT_ROW * 2) + (i16 >> 3) * 64 + g4 * 16;
      if (!z2) wb_dma(4, wbo + 4 * 3072);
      gst16o_sc1<0>(vo, s1, dst); gst16o_sc1<0>(vo, s2, dst + 8 * (XT_ROW * 2));
      if (mt == 0 && q == 0 && nt < 14 && lane < 32) gst16o_sc1<112>((u32)lane * (XT_ROW * 2), (u32x4){0, 0, 0, 0}, ws + WS_XT + (size_t)(32 * nt) * (XT_ROW * 2));
    }
    if (z2) WAITV(16); else WAITV(19);
    STAMP(20);
    BARRIER();
    STAMP(15);
    wb_dma(5 + z2, wbo + RS + z2 * 3072); wb_dma(7 + z2, wbo + RS + (z2 + 2) * 3072);
    if (!z2) wb_dma(9, wbo + RS + 4 * 3072);
    B_XREAD(5);
    if (z2) WAITV(14); else WAITV(20);
    if (w == 6) {
      PINV(cg0); PINV(cg1);
#pragma nounroll
      for (u32 s = 0; s < SPIN_CAP; ++s) {
        if (__all(cg0.y == T && cg0.w == T && cg1.y == T && cg1.w == T)) break;
        __builtin_amdgcn_s_sleep(1);
        asm volatile("global_load_dwordx4 %0, %2, %3 sc1\n\tglobal_load_dwordx4 %1, %2, %4 sc1\n\ts_waitcnt vmcnt(0)" : "=&v"(cg0), "=&v"(cg1)
                     : "v"(vst), "s"(ws + WS_CS + (size_t)(112 * nt) * 8), "s"(ws + WS_CS + (size_t)(1792 + 112 * nt) * 8) : "memory");
      }
      *(LAS u32x2*)(lds + L_CS + (vst >> 1)) = (u32x2){cg0.x, cg0.z};
      *(LAS u32x2*)(lds + L_CS + 448 + (vst >> 1)) = (u32x2){cg1.x, cg1.z};
    }
    STAMP(21);
    BARRIER();
    wb_dma(10 + z2, wbo + 2 * RS + z2 * 3072); wb_dma(12 + z2, wbo + 2 * RS + (z2 + 2) * 3072);
    B_XREAD(6);
    const u32 stt = *(LAS const u32*)(lds + L_ST + (32 * q + t31) * 4);
    const float sd = bfhi(stt), rs = __builtin_amdgcn_rcpf(sd);
    float k0 = -2.3022081986f * rs, k1 = k0 * 0.044715f * rs * rs, s2 = sd * sd;
    const u32 nm = hh ? 0u : (((stt ^ 0x8000u) & 0xffffu) * 0x10001u);
    u32x4 mb = {nm, 0, 0, 0};
    u32x4 ca[3];
#pragma unroll
    for (int j = 0; j < 3; ++j) {
      const float c = *(LAS const float*)(lds + L_CS + ((g4 & 1) * 112 + 16 * (j < a0 ? j : j + 4) + i16) * 4);
      const float chi = rbf(c);
      ca[j] = (u32x4){pk_bf16(chi, c - chi), 0, 0, 0}; PINV(ca[j]);
    }
    u32 wf[3];
    wf[0] = lbase + RB + (g4 & 1) * WB_UP + (8 * (g4 >> 1) + (i16 >> 2)) * 96 + 8 * (i16 & 3); wf[1] = wf[0] + RS; wf[2] = wf[1] + RS;
    PINV(wf[0]); PINV(wf[1]); PINV(wf[2]); PINV(k0); PINV(k1); PINV(s2); PINV(mb);
    char* hdst = ws + WS_HF + ((size_t)(4 * mt + q) * 112 + 7 * nt) * 1024;
    u32 hvo[3];
#pragma unroll
    for (int j = 0; j < 3; ++j) { hvo[j] = lane * 16 + (j < a0 ? j : j + 4) * 1024; PINV(hvo[j]); }
    WAITV(6);
    if (lane == 0 && nt < 14) gst4_sc1(0, T, ws + WS_FX + (nt * 64 + mt * 4 + q) * 4);
    ld_params(); gld2(dsk0, b * 2, P.dskip); gld2(dsk1, kch * 2, P.dskip);
    STAMP(22);
    BARRIER();
    STAMP(2);
    {
      __builtin_amdgcn_s_setprio(1);
      f32x16 acc[2];
      float p0, p1; u32x4 ho;
      constexpr int PD = 7;
      u32x2 fr[8][2];
#define P2_READ(n) do { LAS const char* p_ = (LAS const char*)(size_t)wf[((n) % 28) / 10] + (((n) % 28) % 10) * 1536 + ((n) / 28) * 32; \
        fr[(n) & 7][0] = trread(p_); fr[(n) & 7][1] = trread(p_ + 384); } while (0)
      static_for<0, PD>([&](auto ic) AINL { P2_READ(ic.value); });
      static_for<0, 84>([&](auto ic) AINL {
        constexpr int n = ic.value, j = n / 28, s = n % 28;
        if constexpr (n == 12) STAMP(28);
        if constexpr (n == 13) STAMP(29);
        if constexpr (n == 56) STAMP(30);
        if constexpr (n == 12) { WAITV(9); asm volatile("s_barrier" ::: "memory"); }
        if constexpr (n + PD < 84) P2_READ(n + PD);
        p2_mmv<4 * s, s == 0>(acc[j & 1], (u32x4){fr[n & 7][0].x, fr[n & 7][0].y, fr[n & 7][1].x, fr[n & 7][1].y});
        if constexpr (s == 27) mfma_corr(acc[j & 1], ca[j], mb);
        if constexpr (j > 0 && s >= 4 && s < 20) {
          constexpr int i = (s - 4) >> 2, g = (s - 4) & 3;
          f32x16& a = acc[(j - 1) & 1];
          if constexpr (g == 0) gg1(a[2 * i], a[2 * i + 1], k0, k1, p0, p1);
          if constexpr (g == 1) gg2(a[2 * i], a[2 * i + 1], p0, p1);
          if constexpr (g == 2) gg3(s2, p0, p1);
          if constexpr (g == 3) ho[i] = gg4(a[2 * i], a[2 * i + 8], a[2 * i + 1], a[2 * i + 9], p0, p1);
        }
        if constexpr (j > 0 && s == 20) gst16o_sc1<0>(hvo[j - 1], ho, hdst);
      });
      NOP16(acc[0]);
      gst16o_sc1<0>(hvo[2], geglu8(acc[0], k0, k1, s2), hdst);
      __builtin_amdgcn_s_setprio(0);
    }
    STAMP(3);
    BARRIER();
    WAITV(0);
    if (lane == 0) gst4_sc1(0, T, ws + WS_FH + ((4 * mt + q) * 32 + 16 + nt) * 4);
    STAMP(4);
    __builtin_amdgcn_s_setprio(3);
    PINV(pr[0]); PINV(pr[1]); PINV(pr[2]); PINV(pr[3]); PINV(pr[4]); PINV(pr[5]); PINV(ldt); PINV(dsk0); PINV(dsk1);
    kgen<true>(pr, ldt, lbase + RB + q * (16 * KG_STR), lbase + L_KP + (2 * q + 1) * KP_SZ, lane);
    {
      const u32 m = 255 - tb, cur = lbase + L_TAB + ktab0(m), nxt = lbase + L_TAB + ktab0(m + 1);
      const u32 sel = m < 32 ? 0x76543210u : (m < 64 ? 0xF3222100u : (m < 128 ? 0xFF111000u : ((m & 1) ? 0xFFFF0000u : 0xFFF00000u)));
#pragma unroll
      for (int e = 0; e < 8; ++e) { const u32 nb = (sel >> (4 * (7 - e))) & 15u; ix[e] = nb == 15u ? nxt : cur + nb * 64u; PINV(ix[e]); }
      ix[8] = m <= 32 ? lbase + L_TAB + 7 * 64 + (m ? m - 1 : 0) * 2 : cur; PINV(ix[8]);
    }
    bsync(lds + L_FLG + 16, lane);
    {
#pragma unroll
      for (int i = 0; i < 2; ++i) {
        const int e = i ? (tb < 80 ? tb + 256 : tb) : tb;
        const int cs = e >= 168, ei = e - 168 * cs, row = ei >> 3, c4 = ei & 7;
        const int po = row < 16 ? ((c4 >> 2) * 4 + (row & 3)) * 256 + ((row >> 2) * 16 + (c4 & 3) * 4) * 4
                                : 2048 + ((c4 >> 2) * 4 + (row & 3)) * 128 + (((row - 16) >> 2) * 16 + (c4 & 3) * 4) * 4;
        LAS const char* p = lds + L_KP + cs * KP_SZ + po;
        const f32x4 p0 = *(LAS const f32x4*)p, p1 = *(LAS const f32x4*)(p + 2 * KP_SZ), p2 = *(LAS const f32x4*)(p + 4 * KP_SZ), p3 = *(LAS const f32x4*)(p + 6 * KP_SZ);
        float s[4];
#pragma unroll
        for (int k = 0; k < 4; ++k) s[k] = fadd1(fadd1(p0[k], p1[k]), fadd1(p2[k], p3[k]));
        if (ei == 0) s[0] += bf2f(cs ? dsk1 : dsk0);
        *(LAS u32x2*)(lds + L_TAB + cs * 1536 + ei * 8) = (u32x2){pk_bf16(s[0], s[1]), pk_bf16(s[2], s[3])};
      }
    }
    bsync(lds + L_FLG + 20, lane);
    {
#pragma unroll
      for (int c2 = 0; c2 < 2; ++c2) {
        u32 v[9];
#pragma unroll
        for (int e = 0; e < 9; ++e) v[e] = *(LAS const u16*)((LAS const char*)(size_t)ix[e] + c2 * 1536);
        if (tb == 255) v[8] = 0;
        *(LAS u32x4*)(lds + L_KI + c2 * KI_CH + tb * 16) = (u32x4){v[0] | (v[1] << 16), v[2] | (v[3] << 16), v[4] | (v[5] << 16), v[6] | (v[7] << 16)};
        *(LAS u32x4*)(lds + L_KI + c2 * KI_CH + KI_SZ + tb * 16) = (u32x4){v[1] | (v[2] << 16), v[3] | (v[4] << 16), v[5] | (v[6] << 16), v[7] | (v[8] << 16)};
      }
      if (tb < 32) *(LAS u32x4*)(lds + L_KI + (tb >> 3) * KI_SZ + 4096 + (tb & 7) * 16) = (u32x4){0, 0, 0, 0};
    }
    bsync(lds + L_FLG + 24, lane);
    STAMP(9);
    const int cs = q >> 1, rho = q & 1;
    const bool have = !cs || have2;
    const int ch = (cs && have2) ? b + 256 : b;
    if (have) {
      f32x16 yv[2];
      const int vv = g4 & 1;
      LAS const char* pa = lds + L_U + (2 * cs + vv) * U_SZ + 144 * (15 + i16 - hh - 8 * rho - 6);
      LAS const char* pk = lds + L_KI + cs * KI_CH + vv * KI_SZ + (4200 - 112 - 2048 - 768) - 8 * i16 - 128 * hh - 1024 * rho;
      constexpr int CD = 3;
      u32x4 ra0[CD], ra1[CD]; u32x2 rk[CD][4];
#define CV_LOAD(st) do { constexpr int gl_ = (st) >> 3, o_ = 16 * (7 - ((st) & 7)); \
      rk[(st) % CD][0] = *(LAS const volatile u32x2*)(pk + 256 * (3 - gl_) + o_ + 2048); \
      rk[(st) % CD][1] = *(LAS const volatile u32x2*)(pk + 256 * (3 - gl_) + o_ + 2048 + 8); \
      ra1[(st) % CD] = *(LAS const volatile u32x4*)(pa + 288 * (3 - gl_) + o_ + 2304); \
      ra0[(st) % CD] = *(LAS const volatile u32x4*)(pa + 288 * (3 - gl_) + o_); \
      rk[(st) % CD][2] = *(LAS const volatile u32x2*)(pk + 256 * (3 - gl_) + o_); \
      rk[(st) % CD][3] = *(LAS const volatile u32x2*)(pk + 256 * (3 - gl_) + o_ + 8); } while (0)
      CV_LOAD(0); CV_LOAD(1);
      static_for<0, 32>([&](auto ic) AINL {
        constexpr int st = ic.value;
        if constexpr (st + CD - 1 < 32) CV_LOAD(st + CD - 1);
        const u32x4 b0 = {rk[st % CD][0].x, rk[st % CD][0].y, rk[st % CD][1].x, rk[st % CD][1].y};
        const u32x4 b1 = {rk[st % CD][2].x, rk[st % CD][2].y, rk[st % CD][3].x, rk[st % CD][3].y};
        if constexpr (st == 0) { MFMA32_0(yv[1], ra1[st % CD], b0); MFMA32_0(yv[0], ra0[st % CD], b0); }
        else { MFMA32(yv[1], ra1[st % CD], b0); MFMA32(yv[0], ra0[st % CD], b0); }
        MFMA32(yv[1], ra0[st % CD], b1);
      });
      asm volatile("s_nop 7\n\ts_nop 7" : "+v"(yv[0]), "+v"(yv[1]));
      STAMP(24);
      LAS f32x4* xc = (LAS f32x4*)(lds + L_XCH + q * 4096) + lane;
#pragma unroll
      for (int g = 0; g < 4; ++g) {
        f32x4 v;
#pragma unroll
        for (int e = 0; e < 4; ++e) v[e] = rho ? yv[1][4 * g + e] : yv[0][4 * g + e];
        xc[64 * g] = v;
      }
      asm volatile("s_waitcnt lgkmcnt(0)" ::: "memory");
      *(LAS volatile u32*)(lds + L_FLG + q * 4) = 1u;
#pragma nounroll
      for (u32 s = 0; s < (SPIN_CAP << 4); ++s) { if (*(LAS volatile u32*)(lds + L_FLG + (q ^ 1) * 4)) break; }
      asm volatile("" ::: "memory");
      STAMP(25);
      LAS const f32x4* xr = (LAS const f32x4*)(lds + L_XCH + (q ^ 1) * 4096) + lane;
      float yy[16];
#pragma unroll
      for (int g = 0; g < 4; ++g) {
        const f32x4 v = xr[64 * g];
#pragma unroll
        for (int e = 0; e < 4; ++e) yy[4 * g + e] = v[e] + (rho ? yv[0][4 * g + e] : yv[1][4 * g + e]);
      }
      char* arow = ws + WS_AT + (size_t)ch * 4096 + (rho ? 0 : 2048);
      const u32 aoff = hh * 512 + vv * 1024 + i16 * 8;
      static_for<0, 4>([&](auto ic) AINL {
        constexpr int k = ic.value;
        const u32 p0 = pk_bf16(yy[k + 8], yy[k]), p1 = pk_bf16(yy[k + 12], yy[k + 4]);
        const auto sw = __builtin_amdgcn_permlane16_swap(p0, p1, false, false);
        gst8_sc1<128 * k>(aoff, (u32x2){__builtin_amdgcn_perm(sw[0], sw[1], 0x05040100u), __builtin_amdgcn_perm(sw[0], sw[1], 0x07060302u)}, arow);
      });
      STAMP(26);
      WAITV(0);
      if (lane == 0) gst4_sc1(0, T, ws + WS_FA + ((rho ? 0 : 448) + ch) * 4);
    }
    STAMP(5);
    if (b == 255 && w == 7) {
#pragma unroll 1
      for (int i = 0; i < 4; ++i) poll_flags((u32)((4 * i + g4) * 128 + i16) * 4, ws + WS_FH, T);
      if (lane == 0) gst4_sc1(0, T, ws + WS_EPOCH);
    }
    u32x4 xv;
    const int id = tid < 448 ? tid : 0;
    const u32 xo = (u32)(id / 7) * 896 + (id % 7) * 16;
    {
      poll_flags((u32)((mt2 >> 4) * 448 + 56 * ct + 14 * q + (lane < 14 ? lane : 0)) * 4, ws + WS_FA, T);
      STAMP(27);
      const char* ab = ws + WS_AT + (size_t)(56 * ct + 14 * q) * 4096 + mt2 * 128;
      const int e1 = lane + 64 < 112 ? lane + 64 : lane;
      u32x4 a0, a1;
      gld16_sc1(a0, (u32)(lane >> 3) * 4096 + (lane & 7) * 16, ab);
      gld16_sc1(a1, (u32)(e1 >> 3) * 4096 + (e1 & 7) * 16, ab);
      gld16(xv, xo, (const char*)P.x + (size_t)mt2 * 64 * 896 + ct * 112);
      WAITV(1);
      PINV(a0); PINV(a1);
      LAS char* p0 = lds + L_BASE + (lane & 7) * 8 * PSTR + (14 * q + (lane >> 3)) * 4;
      LAS char* p1 = lds + L_BASE + (e1 & 7) * 8 * PSTR + (14 * q + (e1 >> 3)) * 4;
#pragma unroll
      for (int k = 0; k < 4; ++k) {
        *(LAS float*)(p0 + (2 * k) * PSTR) = bf2f(a0[k]); *(LAS float*)(p0 + (2 * k + 1) * PSTR) = bfhi(a0[k]);
        *(LAS float*)(p1 + (2 * k) * PSTR) = bf2f(a1[k]); *(LAS float*)(p1 + (2 * k + 1) * PSTR) = bfhi(a1[k]);
      }
    }
    STAMP(6);
    BARRIER();
    WAITV(0);
    PINV(xv);
    if (tid < 448) {
      LAS const char* pp = lds + (id / 7) * PSTR + (id % 7) * 32;
      f32x4 s0 = *(LAS const f32x4*)(pp + L_BASE), s1 = *(LAS const f32x4*)(pp + L_BASE + 16);
#pragma unroll
      for (int p = 0; p < 4; ++p) { s0 += *(LAS const f32x4*)(pp + L_PART + p * PART); s1 += *(LAS const f32x4*)(pp + L_PART + p * PART + 16); }
      u32x4 ov;
      ov.x = pk_bf16(s0[0] + bf2f(xv.x), s0[1] + bfhi(xv.x)); ov.y = pk_bf16(s0[2] + bf2f(xv.y), s0[3] + bfhi(xv.y));
      ov.z = pk_bf16(s1[0] + bf2f(xv.z), s1[1] + bfhi(xv.z)); ov.w = pk_bf16(s1[2] + bf2f(xv.w), s1[3] + bfhi(xv.w));
      *(u32x4*)((char*)P.out + (size_t)mt2 * 64 * 896 + ct * 112 + xo) = ov;
    }
    STAMP(7);
    STAMP_OUT();
  }
}

__global__ __launch_bounds__(512, 2) void fused(const Params P) {
  extern __shared__ __attribute__((aligned(16))) char lds_[];
  const int w = __builtin_amdgcn_readfirstlane(threadIdx.x >> 6);
  if (w >= 4) run<true>(P, (LAS char*)lds_, w); else run<false>(P, (LAS char*)lds_, w);
}

extern "C" void kernel_launch(void* const* d_in, const int* in_sizes, int n_in,
                              void* d_out, int out_size, void* d_ws, size_t ws_size,
                              hipStream_t stream) {
  Params P;
  P.x = (const u16*)d_in[0]; P.Are = (const u16*)d_in[1]; P.Aim = (const u16*)d_in[2]; P.Bre = (const u16*)d_in[3];
  P.Bim = (const u16*)d_in[4]; P.Cre = (const u16*)d_in[5]; P.Cim = (const u16*)d_in[6]; P.logdt = (const u16*)d_in[7];
  P.dskip = (const u16*)d_in[8]; P.wg = (const u16*)d_in[10]; P.wu = (const u16*)d_in[11]; P.wd = (const u16*)d_in[12];
  P.out = (u16*)d_out; P.ws = (char*)d_ws;
  (void)hipFuncSetAttribute((const void*)fused, hipFuncAttributeMaxDynamicSharedMemorySize, LDS_BYTES);
  fused<<<256, 512, LDS_BYTES, stream>>>(P);
}
```

```cpp
#include <hip/hip_runtime.h>
#include <stdint.h>

typedef unsigned short u16;
typedef unsigned int u32;
typedef unsigned long long u64;
typedef __attribute__((ext_vector_type(4))) u32 u32x4;
typedef __attribute__((ext_vector_type(2))) u32 u32x2;
typedef __attribute__((ext_vector_type(4))) float f32x4;
typedef __attribute__((ext_vector_type(16))) float f32x16;
typedef __attribute__((ext_vector_type(8))) __bf16 bf16x8;
typedef __attribute__((ext_vector_type(2))) __bf16 bf16x2;
typedef __attribute__((ext_vector_type(2))) float f32x2;
typedef __attribute__((ext_vector_type(4))) short s16x4;
#define LAS __attribute__((address_space(3)))
#define GAS __attribute__((address_space(1)))
#define AINL __attribute__((always_inline))

static constexpr int D = 448;
static constexpr size_t WS_EPOCH = 0;
static constexpr size_t WS_ST = 4096;
static constexpr size_t WS_CS = WS_ST + 2048 * 8;
static constexpr size_t WS_FX = WS_CS + 3584 * 8;
static constexpr size_t WS_FH = WS_FX + 4096;
static constexpr size_t WS_FW = WS_FH + 8192;
static constexpr size_t WS_HF = 128 << 10;
static constexpr size_t WS_WD = WS_HF + (size_t)64 * 112 * 1024;
static constexpr size_t WS_XT = WS_WD + (size_t)8 * 112 * 1792;
static constexpr int XT_ROW = 2112;
static constexpr size_t WS_AT = WS_XT + (size_t)448 * XT_ROW * 2 + 4096;
static constexpr size_t WS_DUMMY = WS_AT + (size_t)448 * 8192;

static constexpr int RS = 32768;
static constexpr int AUX = 0;
static constexpr int RB = 32768;
static constexpr int L_ST = AUX;
static constexpr int L_CS = AUX + 512;
static constexpr int L_CSP = AUX + 1536;
static constexpr int L_FLG = AUX + 2048;
static constexpr int L_ST2 = AUX + 2304;
static constexpr int KP_SZ = 2688;
static constexpr int KS_SZ = 4608;
static constexpr int L_KS = AUX + 3328;
static constexpr int L_KS2 = L_KS + 4 * KS_SZ;
static constexpr int L_TAB = L_KS2 + KP_SZ;
static constexpr int L_KI = AUX + 3328;
static constexpr int KI_SZ = 4224, KI_CH = 8448;
static constexpr int KG_STR = 288;
static constexpr int L_U = RB + 3 * RS;
static constexpr int U_SZ = 7424;
static constexpr int L_XCH = RB;
static constexpr int PSTR = 240, PART = 16384;
static constexpr int L_PART = RB + RS;
static constexpr int L_BASE = RB + 16384;
static constexpr int WB_UP = 15488;
static constexpr int LDS_BYTES = 163840;
static constexpr int B7B = 28, FXP = 30, FXC = 54;
static constexpr int PR_A2 = 1, PR_A3 = 1, PR_A4 = 1, PR_B2 = 2, PR_B3 = 3;
static constexpr u32 SPIN_CAP = 1u << 18;

__device__ __forceinline__ float bf2f(u32 v) { return __uint_as_float(v << 16); }
__device__ __forceinline__ float bfhi(u32 v) { return __uint_as_float(v & 0xffff0000u); }
__device__ __forceinline__ u32 pk_bf16(float lo, float hi) {
  f32x2 v = {lo, hi}; bf16x2 b = __builtin_convertvector(v, bf16x2); return __builtin_bit_cast(u32, b);
}
__device__ __forceinline__ float rbf(float f) { return __uint_as_float(pk_bf16(f, f) << 16); }
template <int I> struct IC { static constexpr int value = I; };
template <int B, int E, class F> __device__ __forceinline__ void static_for(F&& f) {
  if constexpr (B < E) { f(IC<B>{}); static_for<B + 1, E>(f); }
}
#define WAITV(n) asm volatile("s_waitcnt vmcnt(" #n ")" ::: "memory")
#define BARRIER() asm volatile("s_waitcnt lgkmcnt(0)\n\ts_barrier" ::: "memory")

__device__ __forceinline__ void gld16(u32x4& d, u32 voff, const void* sb) {
  asm volatile("global_load_dwordx4 %0, %1, %2" : "=v"(d) : "v"(voff), "s"(sb) : "memory"); }
__device__ __forceinline__ void gld16_sc1(u32x4& d, u32 voff, const void* sb) {
  asm volatile("global_load_dwordx4 %0, %1, %2 sc1" : "=v"(d) : "v"(voff), "s"(sb) : "memory"); }
__device__ __forceinline__ void gld4(u32& d, u32 voff, const void* sb) {
  asm volatile("global_load_dword %0, %1, %2" : "=v"(d) : "v"(voff), "s"(sb) : "memory"); }
__device__ __forceinline__ void gld4_sc1(u32& d, u32 voff, const void* sb) {
  asm volatile("global_load_dword %0, %1, %2 sc1" : "=v"(d) : "v"(voff), "s"(sb) : "memory"); }
__device__ __forceinline__ void gld2(u32& d, u32 voff, const void* sb) {
  asm volatile("global_load_ushort %0, %1, %2" : "=v"(d) : "v"(voff), "s"(sb) : "memory"); }
template <int R, int OFF> __device__ __forceinline__ void gld16a_sc1(u32 voff, const void* sb) {
  asm volatile("global_load_dwordx4 a[%c2:%c3], %0, %1 offset:%c4 sc1" :: "v"(voff), "s"(sb), "i"(R), "i"(R + 3), "i"(OFF) : "memory"); }
template <int OFF> __device__ __forceinline__ void gst16o_sc1(u32 voff, u32x4 v, void* sb) {
  asm volatile("global_store_dwordx4 %0, %1, %2 offset:%c3 sc1\n\ts_nop 1" :: "v"(voff), "v"(v), "s"(sb), "i"(OFF) : "memory"); }
template <int OFF> __device__ __forceinline__ void dsw_tid(u32 v) { asm volatile("ds_write_addtid_b32 %0 offset:%c1" :: "v"(v), "i"(OFF) : "memory"); }
__device__ __forceinline__ void kg_read(u32x4 (&f)[4], u32 ad) {
  asm volatile("ds_read_b128 %0, %4\n\tds_read_b128 %1, %4 offset:64\n\tds_read_b128 %2, %4 offset:128\n\tds_read_b128 %3, %4 offset:192"
               : "=&v"(f[0]), "=&v"(f[1]), "=&v"(f[2]), "=&v"(f[3]) : "v"(ad) : "memory");
}
#define PINV(x) asm volatile("" : "+v"(x))
template <int OFF> __device__ __forceinline__ void gst8_sc1(u32 voff, u32x2 v, void* sb) {
  asm volatile("global_store_dwordx2 %0, %1, %2 offset:%3 sc1" :: "v"(voff), "v"(v), "s"(sb), "i"(OFF) : "memory"); }
__device__ __forceinline__ void gst4_sc1(u32 voff, u32 v, void* sb) {
  asm volatile("global_store_dword %0, %1, %2 sc1" :: "v"(voff), "v"(v), "s"(sb) : "memory"); }
__device__ __forceinline__ void setm0(u32 v) { asm volatile("s_mov_b32 m0, %0\n\ts_nop 0" :: "s"(v) : "memory"); }
template <int OFF> __device__ __forceinline__ void dma(u32 voff, const void* sb) {
  asm volatile("global_load_lds_dwordx4 %0, %1 offset:%2" :: "v"(voff), "s"(sb), "i"(OFF) : "memory"); }
template <int OFF> __device__ __forceinline__ void dma_sc1(u32 voff, const void* sb) {
  asm volatile("global_load_lds_dwordx4 %0, %1 offset:%2 sc1" :: "v"(voff), "s"(sb), "i"(OFF) : "memory"); }
__device__ __forceinline__ void poll_flags(u32 voff, const void* sb, u32 T) {
#pragma nounroll
  for (u32 s = 0; s < SPIN_CAP; ++s) {
    u32 v;
    asm volatile("global_load_dword %0, %1, %2 sc1\n\ts_waitcnt vmcnt(0)" : "=v"(v) : "v"(voff), "s"(sb) : "memory");
    if (__all(v == T)) break;
    __builtin_amdgcn_s_sleep(2);
  }
}
__device__ __forceinline__ u32x2 trread(LAS const char* p) {
  return __builtin_bit_cast(u32x2, __builtin_amdgcn_ds_read_tr16_b64_v4i16((LAS s16x4*)p));
}
__device__ __forceinline__ void MFMA32(f32x16& acc, const u32x4& A, const u32x4& B) { asm volatile("v_mfma_f32_32x32x16_bf16 %0, %1, %2, %0" : "+v"(acc) : "v"(A), "v"(B)); }
__device__ __forceinline__ void MFMA32_0(f32x16& acc, const u32x4& A, const u32x4& B) { asm volatile("v_mfma_f32_32x32x16_bf16 %0, %1, %2, 0" : "=&v"(acc) : "v"(A), "v"(B)); }
__device__ __forceinline__ void MFMA16(f32x4& acc, const u32x4& A, const u32x4& B) { asm volatile("v_mfma_f32_16x16x32_bf16 %0, %1, %2, %0" : "+v"(acc) : "v"(A), "v"(B)); }
__device__ __forceinline__ void MFMA16_0(f32x4& acc, const u32x4& A, const u32x4& B) { asm volatile("v_mfma_f32_16x16x32_bf16 %0, %1, %2, 0" : "=&v"(acc) : "v"(A), "v"(B)); }
template <int NW, int R, bool FIRST> __device__ __forceinline__ void g2_step(f32x16& o0, f32x16& o1, f32x16& o2, f32x16& o3) {
  if constexpr (FIRST)
    asm volatile("s_waitcnt vmcnt(%c4)\n\tv_mfma_f32_32x32x16_bf16 %0, a[%c5:%c6], a[%c7:%c8], 0\n\tv_mfma_f32_32x32x16_bf16 %1, a[%c5:%c6], a[%c11:%c12], 0\n\t"
                 "v_mfma_f32_32x32x16_bf16 %2, a[%c9:%c10], a[%c7:%c8], 0\n\tv_mfma_f32_32x32x16_bf16 %3, a[%c9:%c10], a[%c11:%c12], 0"
                 : "=v"(o0), "=v"(o1), "=v"(o2), "=v"(o3)
                 : "i"(NW), "i"(R), "i"(R + 3), "i"(R + 4), "i"(R + 7), "i"(R + 8), "i"(R + 11), "i"(R + 12), "i"(R + 15) : "memory");
  else
    asm volatile("s_waitcnt vmcnt(%c4)\n\tv_mfma_f32_32x32x16_bf16 %0, a[%c5:%c6], a[%c7:%c8], %0\n\tv_mfma_f32_32x32x16_bf16 %1, a[%c5:%c6], a[%c11:%c12], %1\n\t"
                 "v_mfma_f32_32x32x16_bf16 %2, a[%c9:%c10], a[%c7:%c8], %2\n\tv_mfma_f32_32x32x16_bf16 %3, a[%c9:%c10], a[%c11:%c12], %3"
                 : "+v"(o0), "+v"(o1), "+v"(o2), "+v"(o3)
                 : "i"(NW), "i"(R), "i"(R + 3), "i"(R + 4), "i"(R + 7), "i"(R + 8), "i"(R + 11), "i"(R + 12), "i"(R + 15) : "memory");
}
constexpr int g2_ops(int t) { return t < 12 ? 4 : (t < 20 ? 2 : 0); }
constexpr int g2_sum(int a, int b) { int r = 0; for (int t = a; t <= b; ++t) r += g2_ops(t); return r; }
constexpr int g2_wait(int s) {
  int n = 63;
  if (s < 16) n = 2 + g2_sum(s - 7, s - 1);
  if (s >= 24) n = 2 + g2_sum(s - 15, s - 1);
  if (s + 1 >= 16 && s + 1 < 28) { const int m = g2_sum(s - 6, s - 1); n = m < n ? m : n; }
  return n;
}
template <int OFF> __device__ __forceinline__ void g2_wread(u32x4& w0, u32x4& w1, u32 a0, u32 a1) {
  asm volatile("ds_read_b128 %0, %2 offset:%c4\n\tds_read_b128 %1, %3 offset:%c4" : "=&v"(w0), "=&v"(w1) : "v"(a0), "v"(a1), "i"(OFF) : "memory");
}
template <int NW, int H, int OFF, bool RD> __device__ __forceinline__ void g2_stepl(f32x16& o0, f32x16& o1, f32x16& o2, f32x16& o3, u32x4& w0, u32x4& w1, u32x4& n0, u32x4& n1, u32 a0, u32 a1) {
  if constexpr (RD)
    asm volatile("s_waitcnt vmcnt(%c10) lgkmcnt(0)\n\tv_mfma_f32_32x32x16_bf16 %0, %6, a[%c11:%c11+3], %0\n\tds_read_b128 %4, %8 offset:%c12\n\tds_read_b128 %5, %9 offset:%c12\n\t"
                 "v_mfma_f32_32x32x16_bf16 %1, %6, a[%c11+8:%c11+11], %1\n\tv_mfma_f32_32x32x16_bf16 %2, %7, a[%c11:%c11+3], %2\n\tv_mfma_f32_32x32x16_bf16 %3, %7, a[%c11+8:%c11+11], %3"
                 : "+v"(o0), "+v"(o1), "+v"(o2), "+v"(o3), "=&v"(n0), "=&v"(n1) : "v"(w0), "v"(w1), "v"(a0), "v"(a1), "i"(NW), "i"(H), "i"(OFF) : "memory");
  else
    asm volatile("s_waitcnt vmcnt(%c6) lgkmcnt(0)\n\tv_mfma_f32_32x32x16_bf16 %0, %4, a[%c7:%c7+3], %0\n\t"
                 "v_mfma_f32_32x32x16_bf16 %1, %4, a[%c7+8:%c7+11], %1\n\tv_mfma_f32_32x32x16_bf16 %2, %5, a[%c7:%c7+3], %2\n\tv_mfma_f32_32x32x16_bf16 %3, %5, a[%c7+8:%c7+11], %3"
                 : "+v"(o0), "+v"(o1), "+v"(o2), "+v"(o3) : "v"(w0), "v"(w1), "i"(NW), "i"(H) : "memory");
}
template <int RB_, bool FIRST> __device__ __forceinline__ void p2_mmv(f32x16& acc, const u32x4& A) {
  if constexpr (FIRST) asm volatile("v_mfma_f32_32x32x16_bf16 %0, %1, a[%c2:%c2+3], 0" : "=&v"(acc) : "v"(A), "i"(RB_) : "memory");
  else asm volatile("v_mfma_f32_32x32x16_bf16 %0, %1, a[%c2:%c2+3], %0" : "+v"(acc) : "v"(A), "i"(RB_) : "memory");
}
template <int R, int SO, int KO> __device__ __forceinline__ void a2_read(u32 ax, u32 w0, u32 w1) {
  asm volatile("ds_read_b128 a[%c3:%c3+3], %0 offset:%c4\n\tds_read_b64_tr_b16 a[%c3+8:%c3+9], %1 offset:%c5\n\tds_read_b64_tr_b16 a[%c3+10:%c3+11], %1 offset:%c5+512\n\t"
               "ds_read_b128 a[%c3+4:%c3+7], %0 offset:%c4+4096\n\tds_read_b64_tr_b16 a[%c3+12:%c3+13], %2 offset:%c5\n\tds_read_b64_tr_b16 a[%c3+14:%c3+15], %2 offset:%c5+512"
               :: "v"(ax), "v"(w0), "v"(w1), "i"(R), "i"(SO), "i"(SO + KO) : "memory");
}
template <int R, int RN, int SO, int KO, bool FIRST, bool RD> __device__ __forceinline__ void a2_step(f32x16& o0, f32x16& o1, f32x16& o2, f32x16& o3, u32 ax, u32 w0, u32 w1) {
#define A2_BODY(C0, C1, C2, C3) \
    "s_waitcnt lgkmcnt(6)\n\tv_mfma_f32_32x32x16_bf16 %0, a[%c7+8:%c7+11], a[%c7:%c7+3], " C0 "\n\t" \
    "ds_read_b128 a[%c8:%c8+3], %4 offset:%c9\n\tds_read_b64_tr_b16 a[%c8+8:%c8+9], %5 offset:%c10\n\t" \
    "v_mfma_f32_32x32x16_bf16 %1, a[%c7+8:%c7+11], a[%c7+4:%c7+7], " C1 "\n\t" \
    "ds_read_b64_tr_b16 a[%c8+10:%c8+11], %5 offset:%c10+512\n\tds_read_b128 a[%c8+4:%c8+7], %4 offset:%c9+4096\n\t" \
    "v_mfma_f32_32x32x16_bf16 %2, a[%c7+12:%c7+15], a[%c7:%c7+3], " C2 "\n\t" \
    "ds_read_b64_tr_b16 a[%c8+12:%c8+13], %6 offset:%c10\n\tds_read_b64_tr_b16 a[%c8+14:%c8+15], %6 offset:%c10+512\n\t" \
    "v_mfma_f32_32x32x16_bf16 %3, a[%c7+12:%c7+15], a[%c7+4:%c7+7], " C3
  if constexpr (!RD)
    asm volatile("s_waitcnt lgkmcnt(0)\n\tv_mfma_f32_32x32x16_bf16 %0, a[%c4+8:%c4+11], a[%c4:%c4+3], %0\n\tv_mfma_f32_32x32x16_bf16 %1, a[%c4+8:%c4+11], a[%c4+4:%c4+7], %1\n\t"
                 "v_mfma_f32_32x32x16_bf16 %2, a[%c4+12:%c4+15], a[%c4:%c4+3], %2\n\tv_mfma_f32_32x32x16_bf16 %3, a[%c4+12:%c4+15], a[%c4+4:%c4+7], %3"
                 : "+v"(o0), "+v"(o1), "+v"(o2), "+v"(o3) : "i"(R) : "memory");
  else if constexpr (FIRST)
    asm volatile(A2_BODY("0", "0", "0", "0") : "=&v"(o0), "=&v"(o1), "=&v"(o2), "=&v"(o3) : "v"(ax), "v"(w0), "v"(w1), "i"(R), "i"(RN), "i"(SO), "i"(SO + KO) : "memory");
  else
    asm volatile(A2_BODY("%0", "%1", "%2", "%3") : "+v"(o0), "+v"(o1), "+v"(o2), "+v"(o3) : "v"(ax), "v"(w0), "v"(w1), "i"(R), "i"(RN), "i"(SO), "i"(SO + KO) : "memory");
}
__device__ __forceinline__ void mfma_corr(f32x16& acc, const u32x4& A, const u32x4& B) { asm volatile("s_nop 1\n\tv_mfma_f32_32x32x16_bf16 %0, %1, %2, %0" : "+v"(acc) : "v"(A), "v"(B)); }
__device__ __forceinline__ void pin4(u32x2& a, u32x2& b, u32x2& c, u32x2& d) { asm volatile("" : "+v"(a), "+v"(b), "+v"(c), "+v"(d)); }
__device__ __forceinline__ void pinu(u32& v) { asm volatile("" : "+v"(v)); }
__device__ __forceinline__ void pin(f32x16& v) { asm volatile("" : "+v"(v)); }
#define NOP16(x) asm volatile("s_nop 7\n\ts_nop 7" : "+v"(x))
#define DPPADD(v, ctrl, rm) v += __builtin_bit_cast(float, __builtin_amdgcn_update_dpp(0, __builtin_bit_cast(int, v), ctrl, rm, 0xf, true))
__device__ __forceinline__ f32x2 cmul(f32x2 z, f32x2 w, f32x2 wc) {
  f32x2 zx = {z.x, z.x}, zy = {z.y, z.y};
  return __builtin_elementwise_fma(zy, wc, zx * w);
}
__device__ __forceinline__ f32x2 cexp(float a2, float brev, float t) {
  const float m = __builtin_amdgcn_exp2f(a2 * t), ph = __builtin_amdgcn_fractf(brev * t);
  return (f32x2){m * __builtin_amdgcn_cosf(ph), m * __builtin_amdgcn_sinf(ph)};
}
__device__ __forceinline__ float ffma1(float a, float b, float c) { float r; asm("v_fma_f32 %0, %1, %2, %3" : "=v"(r) : "v"(a), "v"(b), "v"(c)); return r; }
__device__ __forceinline__ float fadd1(float a, float b) { float r; asm("v_add_f32 %0, %1, %2" : "=v"(r) : "v"(a), "v"(b)); return r; }
__device__ __forceinline__ void gg1(float t0, float t1, float k0, float k1, float& p0, float& p1) {
  asm volatile("v_mul_f32 %0, %2, %2\n\tv_mul_f32 %1, %3, %3\n\tv_fma_f32 %0, %0, %5, %4\n\tv_fma_f32 %1, %1, %5, %4" : "=&v"(p0), "=&v"(p1) : "v"(t0), "v"(t1), "v"(k0), "v"(k1)); }
__device__ __forceinline__ void gg2(float t0, float t1, float& p0, float& p1) {
  asm volatile("v_mul_f32 %0, %0, %2\n\tv_mul_f32 %1, %1, %3\n\tv_exp_f32 %0, %0\n\tv_exp_f32 %1, %1" : "+v"(p0), "+v"(p1) : "v"(t0), "v"(t1)); }
__device__ __forceinline__ void gg3(float s2, float& p0, float& p1) {
  asm volatile("v_fma_f32 %0, %0, %2, %2\n\tv_fma_f32 %1, %1, %2, %2\n\tv_rcp_f32 %0, %0\n\tv_rcp_f32 %1, %1" : "+v"(p0), "+v"(p1) : "v"(s2)); }
__device__ __forceinline__ u32 gg4(float t0, float u0, float t1, float u1, float p0, float p1) {
  float a, b; u32 r;
  asm volatile("v_mul_f32 %0, %3, %4\n\tv_mul_f32 %1, %5, %6\n\tv_mul_f32 %0, %0, %7\n\tv_mul_f32 %1, %1, %8\n\tv_cvt_pk_bf16_f32 %2, %0, %1"
      : "=&v"(a), "=&v"(b), "=v"(r) : "v"(t0), "v"(u0), "v"(t1), "v"(u1), "v"(p0), "v"(p1));
  return r; }
__device__ __forceinline__ u32x4 geglu8(const f32x16& a, float k0, float k1, float s2) {
  u32x4 o;
#pragma unroll
  for (int i = 0; i < 4; ++i) {
    float p0, p1;
    gg1(a[2 * i], a[2 * i + 1], k0, k1, p0, p1); gg2(a[2 * i], a[2 * i + 1], p0, p1); gg3(s2, p0, p1);
    o[i] = gg4(a[2 * i], a[2 * i + 8], a[2 * i + 1], a[2 * i + 9], p0, p1);
  }
  return o;
}
__device__ __forceinline__ u32x4 geglu8p(const f32x16& a, float k0, float k1, float s2) {
  u32x4 o;
  const f32x2 K0 = {k0, k0}, K1 = {k1, k1}, S2 = {s2, s2};
#pragma unroll
  for (int i = 0; i < 4; ++i) {
    const f32x2 t = {a[2 * i], a[2 * i + 1]}, u = {a[2 * i + 8], a[2 * i + 9]};
    const f32x2 x = t * __builtin_elementwise_fma(t * t, K1, K0);
    const f32x2 e = {__builtin_amdgcn_exp2f(x.x), __builtin_amdgcn_exp2f(x.y)};
    const f32x2 d = __builtin_elementwise_fma(e, S2, S2);
    const f32x2 r = {__builtin_amdgcn_rcpf(d.x), __builtin_amdgcn_rcpf(d.y)};
    const f32x2 h = (t * u) * r;
    o[i] = pk_bf16(h.x, h.y);
  }
  return o;
}
template <bool PK> __device__ __forceinline__ f32x2 cm(f32x2 z, f32x2 w) {
  if constexpr (PK) { const f32x2 wc = {-w.y, w.x}; return cmul(z, w, wc); }
  else {
    float re, im;
    asm("v_mul_f32 %0, %2, %4\n\tv_mul_f32 %1, %2, %5\n\tv_fma_f32 %0, -%3, %5, %0\n\tv_fma_f32 %1, %3, %4, %1" : "=&v"(re), "=&v"(im) : "v"(z.x), "v"(z.y), "v"(w.x), "v"(w.y));
    return (f32x2){re, im};
  }
}
template <bool PK, int U, class Y> __device__ __forceinline__ void kgen(const u32 (&pr)[6], float dt, u32 so, f32x4 (&kacc)[2][2], int lane, Y&& yield) {
  const int i16 = lane & 15, g4 = lane >> 4;
#define PRM(i) (U == 1 ? bfhi(pr[i]) : bf2f(pr[i]))
  const float a2 = dt * PRM(0) * 1.4426950408889634f, br = dt * PRM(1) * 0.15915494309189535f;
  const f32x2 c = cm<PK>((f32x2){PRM(4), -PRM(5)}, (f32x2){dt * PRM(2), -dt * PRM(3)});
  const f32x2 e1 = cexp(a2, -br, 1.f), e256 = cexp(a2, -br, 256.f), e8 = cexp(a2, br, 8.f), e64 = cexp(a2, br, 64.f), e128 = cexp(a2, br, 128.f);
  const f32x2 e2 = cm<PK>(e1, e1), e4 = cm<PK>(e2, e2);
  const u32 rp = so + i16 * KG_STR + g4 * 16;
  const u32 rp5 = so + (i16 < 5 ? i16 : 4) * KG_STR + g4 * 16;
#define KW(row, z) dsw_tid<(row) * KG_STR>(pk_bf16((z).x, (z).y))
  u32x4 fa0[4], fa1[4], fb[4];
  {
    f32x2 z = c, y = cm<PK>(c, e256);
    const f32x2 a512 = cm<PK>(y, e256), a768 = cm<PK>(a512, e256);
    f32x2 u = cm<PK>(a768, e256);
    static_for<0, 8>([&](auto ic) AINL {
      constexpr int k = ic.value;
      if constexpr (k > 0) z = cm<PK>(z, e1);
      KW(k, z);
      if constexpr (k > 0 && k < 4) y = cm<PK>(y, e2);
      if constexpr (k == 4) y = a512;
      if constexpr (k == 5) y = cm<PK>(a512, e4);
      if constexpr (k == 6) y = a768;
      if constexpr (k == 7) y = cm<PK>(a768, e4);
      KW(8 + k, y);
      yield(IC<10 + k>{});
    });
    kg_read(fa0, rp);
    yield(IC<0>{});
    static_for<0, 5>([&](auto ic) AINL { KW(ic.value, u); if constexpr (ic.value < 4) u = cm<PK>(u, e256); });
    kg_read(fa1, rp5);
    yield(IC<1>{});
  }
  static_for<0, 2>([&](auto hc) AINL {
    constexpr int hb = hc.value;
    f32x2 z = hb ? e128 : (f32x2){1.f, 0.f}, y = hb ? cm<PK>(e128, e64) : e64;
    static_for<0, 8>([&](auto ic) AINL {
      constexpr int k = ic.value;
      if constexpr (k > 0) { z = cm<PK>(z, e8); y = cm<PK>(y, e8); }
      KW(k, z); KW(8 + k, y);
      yield(IC<18 + 8 * hb + k>{});
    });
    if constexpr (hb == 1) {
      asm volatile("s_waitcnt lgkmcnt(15)" : "+v"(fa0[0]), "+v"(fa0[1]), "+v"(fa0[2]), "+v"(fa0[3]), "+v"(fa1[0]), "+v"(fa1[1]), "+v"(fa1[2]), "+v"(fa1[3]),
                   "+v"(fb[0]), "+v"(fb[1]), "+v"(fb[2]), "+v"(fb[3]) :: "memory");
      if constexpr (U == 0) { MFMA16_0(kacc[0][0], fa0[0], fb[0]); MFMA16_0(kacc[1][0], fa1[0], fb[0]); }
      else { MFMA16(kacc[0][0], fa0[0], fb[0]); MFMA16(kacc[1][0], fa1[0], fb[0]); }
#pragma unroll
      for (int ks = 1; ks < 4; ++ks) { MFMA16(kacc[0][0], fa0[ks], fb[ks]); MFMA16(kacc[1][0], fa1[ks], fb[ks]); }
    }
    kg_read(fb, rp);
    yield(IC<2 + hb>{});
  });
  asm volatile("s_waitcnt lgkmcnt(0)" : "+v"(fb[0]), "+v"(fb[1]), "+v"(fb[2]), "+v"(fb[3]) :: "memory");
  if constexpr (U == 0) { MFMA16_0(kacc[0][1], fa0[0], fb[0]); MFMA16_0(kacc[1][1], fa1[0], fb[0]); }
  else { MFMA16(kacc[0][1], fa0[0], fb[0]); MFMA16(kacc[1][1], fa1[0], fb[0]); }
#pragma unroll
  for (int ks = 1; ks < 4; ++ks) { MFMA16(kacc[0][1], fa0[ks], fb[ks]); MFMA16(kacc[1][1], fa1[ks], fb[ks]); }
}
__device__ __forceinline__ void kgen_out(f32x4 (&kacc)[2][2], u32 po) {
  setm0(po);
  asm volatile("ds_write_addtid_b32 %0\n\tds_write_addtid_b32 %1 offset:256\n\tds_write_addtid_b32 %2 offset:512\n\tds_write_addtid_b32 %3 offset:768\n\t"
               "s_mov_b32 exec_hi, 0\n\tds_write_addtid_b32 %4 offset:2048\n\ts_mov_b32 exec_lo, 0xffff\n\t"
               "ds_write_addtid_b32 %5 offset:2176\n\tds_write_addtid_b32 %6 offset:2240\n\tds_write_addtid_b32 %7 offset:2304\n\t"
               "s_mov_b64 exec, -1\n\ts_nop 3"
               :: "v"(kacc[0][0][0]), "v"(kacc[0][0][1]), "v"(kacc[0][0][2]), "v"(kacc[0][0][3]), "v"(kacc[1][0][0]), "v"(kacc[1][0][1]), "v"(kacc[1][0][2]), "v"(kacc[1][0][3]) : "memory");
  asm volatile("ds_write_addtid_b32 %0 offset:1024\n\tds_write_addtid_b32 %1 offset:1280\n\tds_write_addtid_b32 %2 offset:1536\n\tds_write_addtid_b32 %3 offset:1792\n\t"
               "s_mov_b32 exec_hi, 0\n\tds_write_addtid_b32 %4 offset:2368\n\ts_mov_b32 exec_lo, 0xffff\n\t"
               "ds_write_addtid_b32 %5 offset:2496\n\tds_write_addtid_b32 %6 offset:2560\n\tds_write_addtid_b32 %7 offset:2624\n\t"
               "s_mov_b64 exec, -1"
               :: "v"(kacc[0][1][0]), "v"(kacc[0][1][1]), "v"(kacc[0][1][2]), "v"(kacc[0][1][3]), "v"(kacc[1][1][0]), "v"(kacc[1][1][1]), "v"(kacc[1][1][2]), "v"(kacc[1][1][3]) : "memory");
}
__device__ __forceinline__ void bsync(LAS char* cnt, int lane) {
  asm volatile("s_waitcnt lgkmcnt(0)" ::: "memory");
  if (lane == 0) __hip_atomic_fetch_add((LAS u32*)cnt, 1u, __ATOMIC_RELAXED, __HIP_MEMORY_SCOPE_WORKGROUP);
#pragma nounroll
  for (u32 s = 0; s < (SPIN_CAP << 4); ++s) { if (*(LAS volatile u32*)cnt >= 4u) break; }
  asm volatile("" ::: "memory");
}

struct KTab { u16 v[256][16]; };
constexpr u32 ktab0c(u32 m) { const u32 hi = m >> 5; const u32 r0 = hi == 0 ? 0u : (hi < 4 ? (hi == 1 ? 8u : 8u + 2u * hi) : 12u + hi); return r0 * 64u + (m & 31u) * 2u; }
constexpr KTab make_ktab() {
  KTab t{};
  for (u32 tid = 0; tid < 256; ++tid) {
    const u32 m = 255 - tid, cur = L_TAB + ktab0c(m), nxt = L_TAB + ktab0c(m + 1);
    const u32 sel = m < 32 ? 0x76543210u : (m < 64 ? 0xF3222100u : (m < 128 ? 0xFF111000u : ((m & 1) ? 0xFFFF0000u : 0xFFF00000u)));
    for (int e = 0; e < 8; ++e) { const u32 nb = (sel >> (4 * (7 - e))) & 15u; t.v[tid][e] = (u16)(nb == 15u ? nxt : cur + nb * 64u); }
    t.v[tid][8] = (u16)(m <= 32 ? L_TAB + 7 * 64 + (m ? m - 1 : 0) * 2 : cur);
    for (int i = 0; i < 2; ++i) {
      const int e = i ? (tid < 80 ? tid + 256 : tid) : tid;
      const int cs = e >= 168, ei = e - 168 * cs, row = ei >> 3, c4 = ei & 7;
      const int po = row < 16 ? ((c4 >> 2) * 4 + (row & 3)) * 256 + ((row >> 2) * 16 + (c4 & 3) * 4) * 4
                              : 2048 + (c4 >> 2) * 320 + (row == 16 ? 0 : (row == 20 ? 64 : 64 * (row - 15))) + (c4 & 3) * 16;
      t.v[tid][9 + i] = (u16)((cs ? L_KS2 : L_KS) + po); t.v[tid][11 + i] = (u16)(L_TAB + cs * KS_SZ + ei * 8);
    }
  }
  return t;
}
__device__ const KTab g_ktab = make_ktab();

struct Params {
  const u16 *x, *Are, *Aim, *Bre, *Bim, *Cre, *Cim, *logdt, *dskip, *wg, *wu, *wd;
  u16* out; char* ws;
};


template <bool grpB> __device__ __forceinline__ void run(const Params& P, LAS char* lds, const int w) {
  const u32 lbase = (u32)(size_t)lds;
  const int tid = threadIdx.x, lane = tid & 63;
  const int q = w & 3;
  const int b = blockIdx.x, xcd = b & 7, rr = b >> 3;
  const int mt = (xcd >> 2) * 8 + (rr >> 2), nt = (xcd & 3) * 4 + (rr & 3);
  const int mt2 = xcd * 4 + (rr >> 3), ct = rr & 7;
  const int c0 = nt >> 1 < 7 ? nt >> 1 : 0;
  const bool have2 = b + 256 < D;
  const int a0 = nt & 3;
  const int t31 = lane & 31, hh = lane >> 5, g4 = lane >> 4, i16 = lane & 15;
  asm volatile("" ::: "a0", "a127");
  const u32 xfa = (u32)(((grpB ? 32 * q : 64 * (q >> 1)) + t31) * 128 + ((hh ^ ((t31 >> 1) & 7)) * 16));
  u32 xa[2][4];
#pragma unroll
  for (int k = 0; k < 4; ++k) { xa[0][k] = lbase + RB + (xfa ^ (k * 32)); PINV(xa[0][k]); xa[1][k] = xa[0][k] + 2 * RS; PINV(xa[1][k]); }
  u32 vst = (u32)(lane < 56 ? lane : 55) * 16; PINV(vst);
  u32 ep, T, pr[6], ldt;
  auto finish = [&](char* ws_, int id, u32 xo, const u32x4& xv) AINL {
    LAS const char* pp = lds + (id / 7) * PSTR + (id % 7) * 32;
    f32x4 s0 = *(LAS const f32x4*)(pp + L_BASE), s1 = *(LAS const f32x4*)(pp + L_BASE + 16);
#pragma unroll
    for (int p = 0; p < 4; ++p) { s0 += *(LAS const f32x4*)(pp + L_PART + p * PART); s1 += *(LAS const f32x4*)(pp + L_PART + p * PART + 16); }
    u32x4 ov;
    ov.x = pk_bf16(s0[0] + bf2f(xv.x), s0[1] + bfhi(xv.x)); ov.y = pk_bf16(s0[2] + bf2f(xv.y), s0[3] + bfhi(xv.y));
    ov.z = pk_bf16(s1[0] + bf2f(xv.z), s1[1] + bfhi(xv.z)); ov.w = pk_bf16(s1[2] + bf2f(xv.w), s1[3] + bfhi(xv.w));
    *(u32x4*)((char*)P.out + (size_t)mt2 * 64 * 896 + ct * 112 + xo) = ov;
  };

  if constexpr (!grpB) {
    __builtin_amdgcn_s_setprio(3);
    u32 aw[2][2];
    const u32 wfa = (u32)(16384 + (g4 & 1) * 8192 + (8 * (g4 >> 1) + (i16 >> 2)) * 128 + ((((i16 >> 3) & 1) + 2 * (g4 & 1)) * 32) + 8 * (i16 & 3));
#pragma unroll
    for (int k = 0; k < 2; ++k) { aw[0][k] = lbase + RB + (wfa ^ ((2 * (q & 1) + k) * 32)); aw[1][k] = aw[0][k] + 2 * RS; PINV(aw[0][k]); PINV(aw[1][k]); }
    u32 vcs = (u32)(lane >> 1) * 3584 + (lane & 1) * 12, vcs2 = (u32)((lane >> 1) & 15) * 3584 + (lane & 1) * 12; PINV(vcs); PINV(vcs2);
    u32 vwd; { const int l = lane < 56 ? lane : 55, r = (l * 37) >> 9; vwd = (u32)r * 896 + (l - 14 * r) * 16; PINV(vwd); }
    u32 vpr = (u32)(b * 256 + q * 64 + lane) * 2; PINV(vpr);
    u32 vxs = (u32)hh * 896 + (t31 < 28 ? t31 : 27) * 32; PINV(vxs);
    if (tid < 2) { *(LAS u32x4*)(lds + L_FLG + tid * 16) = (u32x4){0, 0, 0, 0}; }
    char* ws = P.ws;
    u32x4 xs[2], cv[4], wdv[2];
    gld4_sc1(ep, 0, ws + WS_EPOCH);
    {
      const char* xb = (const char*)P.x + (size_t)(128 * mt + 8 * nt + 2 * q) * 896;
      gld16(xs[0], vxs, xb); gld16(xs[1], vxs, xb + 16);
    }
    gld2(pr[0], vpr, P.Are); gld2(pr[1], vpr, P.Aim); gld2(pr[2], vpr, P.Bre);
    gld2(pr[3], vpr, P.Bim); gld2(pr[4], vpr, P.Cre); gld2(pr[5], vpr, P.Cim);
    gld2(ldt, 0, (const char*)P.logdt + b * 2);
    WAITV(7);
    PINV(ep); PINV(xs[0]); PINV(xs[1]);
    T = __builtin_amdgcn_readfirstlane(ep) + 1u;
    {
      const bf16x2 one2 = __builtin_bit_cast(bf16x2, 0x3f803f80u);
      float s1 = 0.f, s2 = 0.f;
#pragma unroll
      for (int i = 0; i < 8; ++i) {
        const bf16x2 v = __builtin_bit_cast(bf16x2, t31 < 28 ? xs[i >> 2][i & 3] : 0u);
        s1 = __builtin_amdgcn_fdot2_f32_bf16(v, one2, s1, false); s2 = __builtin_amdgcn_fdot2_f32_bf16(v, v, s2, false);
      }
      auto r = __builtin_amdgcn_permlane16_swap(__float_as_uint(s1), __float_as_uint(s2), false, false);
      float s = __uint_as_float(r[0]) + __uint_as_float(r[1]);
      DPPADD(s, 0x128, 0xf); DPPADD(s, 0x124, 0xf); DPPADD(s, 0x122, 0xf); DPPADD(s, 0x121, 0xf);
      auto t = __builtin_amdgcn_permlane16_swap(__float_as_uint(s), __float_as_uint(s), false, false);
      const float mean = __uint_as_float(t[0]) * (1.f / 448.f), msq = __uint_as_float(t[1]) * (1.f / 448.f);
      const float var = rbf(fmaxf(fmaf(-mean, mean, msq), 0.f));
      const float sd = __builtin_amdgcn_sqrtf(rbf(var + 1e-5f));
      const u32x2 g = {pk_bf16(mean, sd), T};
      if (t31 == 0) gst8_sc1<0>(hh * 8, g, ws + WS_ST + (size_t)(128 * mt + 8 * nt + 2 * q) * 8);
    }
    WAITV(1);
    PINV(pr[0]); PINV(pr[1]); PINV(pr[2]); PINV(pr[3]); PINV(pr[4]); PINV(pr[5]); PINV(ldt);
    {
      const float dt = rbf(__builtin_amdgcn_exp2f(bf2f(ldt) * 1.4426950408889634f));
      f32x4 kacc[2][2];
      setm0(lbase + L_KS + q * KS_SZ);
      kgen<true, 0>(pr, dt, lbase + L_KS + q * KS_SZ, kacc, lane, [&](auto yc) AINL {
        if constexpr (yc.value == 0) {
          const int ub = b < 224 ? b : 0;
          const char* wb_ = (const char*)P.wd + (size_t)(16 * (ub >> 1) + 4 * (ub & 1)) * 896 + 224 * q;
          gld16(wdv[0], vwd, wb_); gld16(wdv[1], vwd, wb_ + 8 * 896);
        }
        if constexpr (yc.value == 1 || yc.value == 2) {
          const char* cb = (const char*)(mt < 8 ? P.wg : P.wu) + (size_t)(112 * q + 64 * (yc.value - 1)) * 3584 + 224 * nt + 28 * (mt & 7);
          gld16(cv[2 * (yc.value - 1)], vcs, cb); gld16(cv[2 * (yc.value - 1) + 1], yc.value == 2 ? vcs2 : vcs, cb + 32 * 3584);
        }
      });
      kgen_out(kacc, lbase + L_KS + q * KS_SZ);
    }
    WAITV(4);
    PINV(wdv[0]); PINV(wdv[1]);
    {
      LAS char* tp = lds + L_KS + q * KS_SZ + KP_SZ;
      const int l = lane < 56 ? lane : 55, r = (l * 37) >> 9;
      *(LAS u32x4*)(tp + r * 240 + (l - 14 * r) * 16) = wdv[0]; *(LAS u32x4*)(tp + 960 + r * 240 + (l - 14 * r) * 16) = wdv[1];
      LAS const char* rp = tp + (i16 >> 2) * 240 + g4 * 32 + (i16 & 3) * 8;
      const u32x2 t0 = trread(rp), t1 = trread(rp + 960), t2 = trread(rp + 112), t3 = trread(rp + 960 + 112);
      if (lane < 56) {
        char* dst = b < 224 ? ws + WS_WD + ((size_t)(2 * q) * 112 * 2 + b) * 896 : ws + WS_DUMMY + (size_t)q * (256 << 10);
        gst16o_sc1<0>(vst, (u32x4){t0.x, t0.y, t1.x, t1.y}, dst);
        gst16o_sc1<0>(vst, (u32x4){t2.x, t2.y, t3.x, t3.y}, dst + 112 * 1792);
      }
    }
    WAITV(2);
    {
      const bf16x2 lo1 = __builtin_bit_cast(bf16x2, 0x3f803f80u), hi1 = __builtin_bit_cast(bf16x2, 0x3f800000u);
      float sa[4], sb[4];
      PINV(cv[0]); PINV(cv[1]); PINV(cv[2]); PINV(cv[3]);
#pragma unroll
      for (int k = 0; k < 4; ++k) {
        sa[k] = 0.f; sb[k] = 0.f;
#pragma unroll
        for (int i = 0; i < 4; ++i) {
          const bf16x2 v = __builtin_bit_cast(bf16x2, (i == 3 && hh) ? 0u : cv[i][k]);
          sa[k] = __builtin_amdgcn_fdot2_f32_bf16(v, lo1, sa[k], false); sb[k] = __builtin_amdgcn_fdot2_f32_bf16(v, hi1, sb[k], false);
        }
        sa[k] -= sb[k];
        auto r = __builtin_amdgcn_permlane32_swap(__float_as_uint(sa[k]), __float_as_uint(sb[k]), false, false);
        sa[k] = __uint_as_float(r[0]) + __uint_as_float(r[1]);
      }
      auto r0 = __builtin_amdgcn_permlane16_swap(__float_as_uint(sa[0]), __float_as_uint(sa[1]), false, false);
      auto r1 = __builtin_amdgcn_permlane16_swap(__float_as_uint(sa[2]), __float_as_uint(sa[3]), false, false);
      float u0 = __uint_as_float(r0[0]) + __uint_as_float(r0[1]), u1 = __uint_as_float(r1[0]) + __uint_as_float(r1[1]);
      DPPADD(u0, 0x128, 0xf); DPPADD(u1, 0x128, 0xf); DPPADD(u0, 0x124, 0xf); DPPADD(u1, 0x124, 0xf); DPPADD(u0, 0x122, 0xf); DPPADD(u1, 0x122, 0xf);
      LAS char* cp = lds + L_CSP + (q * 16 + (lane & 1) * 6 + (g4 & 1) * 2 + hh) * 4;
      *(LAS float*)cp = u0; *(LAS float*)(cp + 16) = u1;
    }
    f32x16 acc[4];
    BARRIER();
    __builtin_amdgcn_s_setprio(1);
#define A_AD(n) xa[((((n) >> 2) + 1) & 3) >> 1][(n) & 3], aw[((((n) >> 2) + 1) & 3) >> 1][0], aw[((((n) >> 2) + 1) & 3) >> 1][1]
#define A_SO(n) (((((n) >> 2) + 1) & 1) * RS)
    u32x4 kt0, kt1;
    a2_read<0, A_SO(0), 0>(A_AD(0)); a2_read<16, A_SO(1), 2048>(A_AD(1));
    static_for<0, 28>([&](auto ic) AINL {
      constexpr int n = ic.value;
      if constexpr (n == 17) { gld16(kt0, tid * 32, &g_ktab); gld16(kt1, tid * 32 + 16, &g_ktab); }
      if constexpr (n == 13) { WAITV(0); if (lane == 0 && b < 224) gst4_sc1(0, T, ws + WS_FW + (q * 224 + b) * 4); }
      if constexpr ((n & 3) == 2) { BARRIER(); }
      a2_step<16 * (n % 3), 16 * ((n + 2) % 3), A_SO(n + 2), ((n + 2) & 3) * 2048, n == 0, (n + 2 < 28)>(acc[0], acc[1], acc[2], acc[3], A_AD(n + 2));
    });
    __builtin_amdgcn_s_setprio(PR_A2);
    u32 fwv;
    const u32 fwo = (u32)((ct >> 1) * 224 + 56 * q + (lane < 56 ? lane : 0)) * 4;
    u32 dsk0, dsk1;
    gld2(dsk0, 0, (const char*)P.dskip + b * 2); gld2(dsk1, 0, (const char*)P.dskip + (have2 ? b + 256 : b) * 2);
    gld4_sc1(fwv, fwo, ws + WS_FW);
    float k0[2], k1[2], s2[2];
    u32 nm[2];
#pragma unroll
    for (int t = 0; t < 2; ++t) {
      const u32 stt = *(LAS const u32*)(lds + L_ST + (64 * (q >> 1) + 32 * t + t31) * 4);
      const float sd = bfhi(stt), rs = __builtin_amdgcn_rcpf(sd);
      k0[t] = -2.3022081986f * rs; k1[t] = k0[t] * 0.044715f * rs * rs; s2[t] = sd * sd;
      nm[t] = hh ? 0u : (((stt ^ 0x8000u) & 0xffffu) * 0x10001u);
    }
#pragma unroll
    for (int f = 0; f < 2; ++f) {
      const float c = *(LAS const float*)(lds + L_CS + ((g4 & 1) * 112 + 16 * (a0 + 2 * (q & 1) + f) + i16) * 4);
      const float chi = rbf(c);
      const u32x4 ca = {pk_bf16(chi, c - chi), 0, 0, 0};
      mfma_corr(acc[2 * f], ca, (u32x4){nm[0], 0, 0, 0}); mfma_corr(acc[2 * f + 1], ca, (u32x4){nm[1], 0, 0, 0});
    }
    BARRIER();
    const char* wb = ws + WS_WD + ((size_t)ct * 112 + 28 * q) * 1792 + 2688;
    const char* hb0 = ws + WS_HF + ((size_t)(2 * mt2) * 112 + 28 * q) * 1024;
    const char* hb1 = hb0 + 112 * 1024;
    const u32 hoff = lane * 16;
    const u32 woff0 = hh * 896 + t31 * 16;
    const u32 woff1 = hh * 896 + (t31 < 24 ? 32 + t31 : 55) * 16;
#define LDW(s) do { gld16a_sc1<16 * (s), ((s) & 3) * 1792 - 2688>(woff0, wb + ((s) & ~3) * 1792); gld16a_sc1<16 * (s) + 8, ((s) & 3) * 1792 - 2688>(woff1, wb + ((s) & ~3) * 1792); } while (0)
#define HR(s) (16 * ((s) & 7) + (((s) >= 16 && (s) < 24) ? 0 : 4))
#define LDH(s) do { gld16a_sc1<HR(s), ((s) & 3) * 1024>(hoff, hb0 + ((s) & ~3) * 1024); gld16a_sc1<HR(s) + 8, ((s) & 3) * 1024>(hoff, hb1 + ((s) & ~3) * 1024); } while (0)
#define DMW(s) do { setm0(lbase + L_PART + q * PART + ((s) & 7) * 2048); dma_sc1<0>(hoff, wb - 2688 + (s) * 1792); dma_sc1<1024>(hoff, wb - 2688 + (s) * 1792); } while (0)
    char* hdst = ws + WS_HF + ((size_t)(4 * mt + (q & 2)) * 112 + 7 * nt + a0 + 2 * (q & 1)) * 1024;
    gst16o_sc1<0>(hoff, geglu8(acc[0], k0[0], k1[0], s2[0]), hdst);
    pin(acc[1]); gst16o_sc1<0>(hoff, geglu8(acc[1], k0[1], k1[1], s2[1]), hdst + 112 * 1024);
    pin(acc[2]); gst16o_sc1<1024>(hoff, geglu8(acc[2], k0[0], k1[0], s2[0]), hdst);
    pin(acc[3]); gst16o_sc1<1024>(hoff, geglu8(acc[3], k0[1], k1[1], s2[1]), hdst + 112 * 1024);
    WAITV(4); PINV(fwv);
    PINV(dsk0); PINV(dsk1); PINV(kt0); PINV(kt1);
    const u32 ix[9] = {lbase + (kt0.x & 0xffffu), lbase + (kt0.x >> 16), lbase + (kt0.y & 0xffffu), lbase + (kt0.y >> 16), lbase + (kt0.z & 0xffffu),
                       lbase + (kt0.z >> 16), lbase + (kt0.w & 0xffffu), lbase + (kt0.w >> 16), lbase + (kt1.x & 0xffffu)};
    const u32 cpo[2] = {lbase + (kt1.x >> 16), lbase + (kt1.y & 0xffffu)}, cto[2] = {lbase + (kt1.y >> 16), lbase + (kt1.z & 0xffffu)};
#pragma unroll
    for (int i = 0; i < 2; ++i) {
      LAS const char* p = (LAS const char*)(size_t)cpo[i];
      const bool c2 = i ? tid < 80 || tid >= 168 : tid >= 168;
      const f32x4 p0 = *(LAS const f32x4*)p, p1 = *(LAS const f32x4*)(p + KS_SZ), p2 = *(LAS const f32x4*)(p + 2 * KS_SZ), p3 = *(LAS const f32x4*)(p + 3 * KS_SZ);
      float s[4];
#pragma unroll
      for (int k = 0; k < 4; ++k) { const float r = fadd1(p2[k], p3[k]); s[k] = fadd1(fadd1(p0[k], p1[k]), c2 ? 0.f : r); }
      if (tid == 0 && i == 0) s[0] += bf2f(dsk0);
      if (tid == 168) s[0] += bf2f(dsk1);
      *(LAS u32x2*)(size_t)cto[i] = (u32x2){pk_bf16(s[0], s[1]), pk_bf16(s[2], s[3])};
    }
    WAITV(0);
    if (lane == 0) gst4_sc1(0, T, ws + WS_FH + ((4 * mt + q) * 32 + nt) * 4);
    if (!__all(fwv == T)) poll_flags(fwo, ws + WS_FW, T);
    static_for<0, 8>([&](auto ic) AINL { LDW(ic.value); });
    bsync(lds + L_FLG + 24, lane);
#pragma unroll
    for (int c2 = 0; c2 < 2; ++c2) {
      u32 v[9];
#pragma unroll
      for (int e = 0; e < 9; ++e) v[e] = *(LAS const u16*)((LAS const char*)(size_t)ix[e] + c2 * KS_SZ);
      if (tid == 255) v[8] = 0;
      *(LAS u32x4*)(lds + L_KI + c2 * KI_CH + tid * 16) = (u32x4){v[0] | (v[1] << 16), v[2] | (v[3] << 16), v[4] | (v[5] << 16), v[6] | (v[7] << 16)};
      *(LAS u32x4*)(lds + L_KI + c2 * KI_CH + KI_SZ + tid * 16) = (u32x4){v[1] | (v[2] << 16), v[3] | (v[4] << 16), v[5] | (v[6] << 16), v[7] | (v[8] << 16)};
    }
    if (tid < 32) *(LAS u32x4*)(lds + L_KI + (tid >> 3) * KI_SZ + 4096 + (tid & 7) * 16) = (u32x4){0, 0, 0, 0};
    asm volatile("s_waitcnt lgkmcnt(0)" ::: "memory");
    if (lane == 0) __hip_atomic_fetch_add((LAS u32*)(lds + L_FLG + 20), 1u, __ATOMIC_RELAXED, __HIP_MEMORY_SCOPE_WORKGROUP);
#pragma nounroll
    for (u32 s = 0; s < (SPIN_CAP << 4); ++s) { if (*(LAS volatile u32*)(lds + L_FLG + 28) >= 4u) break; __builtin_amdgcn_s_sleep(1); }
    asm volatile("" ::: "memory");
    static_for<8, 16>([&](auto ic) AINL { DMW(ic.value); });
    u32x4 xv;
    const u32 xo = (u32)(tid / 7) * 896 + (tid % 7) * 16;
    gld16(xv, xo, (const char*)P.x + (size_t)mt2 * 64 * 896 + ct * 112);
    poll_flags((u32)((2 * mt2 + ((lane >> 3) & 1)) * 32 + ((lane >> 2) & 1) * 16 + 4 * q + (lane & 3)) * 4, ws + WS_FH, T);
    PINV(xv);
    __builtin_amdgcn_s_setprio(PR_A3);
    static_for<0, 8>([&](auto ic) AINL { LDH(ic.value); });
    f32x16 o[4];
    u32x4 wv[2][2];
    const u32 wr0 = lbase + L_PART + q * PART + woff0, wr1 = lbase + L_PART + q * PART + woff1;
    static_for<0, 28>([&](auto ic) AINL {
      constexpr int s = ic.value;
      if constexpr (s < 8) {
        if constexpr (s == 7) g2_wread<0>(wv[0][0], wv[0][1], wr0, wr1);
        g2_step<14 + 2 * s, 16 * s, s == 0>(o[0], o[1], o[2], o[3]);
        LDH(s + 8); LDH(s + 16);
      } else {
        g2_stepl<g2_wait(s), HR(s), ((s + 1) & 7) * 2048, (s < 27)>(o[0], o[1], o[2], o[3], wv[s & 1][0], wv[s & 1][1], wv[(s + 1) & 1][0], wv[(s + 1) & 1][1], wr0, wr1);
        if constexpr (s < 12) LDH(s + 16);
        if constexpr (s < 20) DMW(s + 8);
      }
      if constexpr (s == 7) __builtin_amdgcn_s_setprio(PR_A4);
    });
    NOP16(o[3]);
#pragma unroll
    for (int cb = 0; cb < 2; ++cb)
#pragma unroll
      for (int tt = 0; tt < 2; ++tt) {
        pin(o[cb * 2 + tt]);
        LAS char* pp = lds + L_PART + q * PART + (tt * 32 + t31) * PSTR + (cb * 32 + 4 * hh) * 4;
#pragma unroll
        for (int g = 0; g < 4; ++g) {
          if (cb == 1 && g == 3) continue;
          *(LAS f32x4*)(pp + g * 32) = (f32x4){o[cb * 2 + tt][4 * g], o[cb * 2 + tt][4 * g + 1], o[cb * 2 + tt][4 * g + 2], o[cb * 2 + tt][4 * g + 3]};
        }
      }
    BARRIER();
    finish(ws, tid, xo, xv);
  } else {
#define B_XREAD(st) asm volatile("ds_read_b128 a[%c4:%c4+3], %0 offset:%c5\n\tds_read_b128 a[%c4+4:%c4+7], %1 offset:%c5\n\t" \
      "ds_read_b128 a[%c4+8:%c4+11], %2 offset:%c5\n\tds_read_b128 a[%c4+12:%c4+15], %3 offset:%c5" \
      :: "v"(xa[(((st) + 1) >> 1) & 1][0]), "v"(xa[(((st) + 1) >> 1) & 1][1]), "v"(xa[(((st) + 1) >> 1) & 1][2]), "v"(xa[(((st) + 1) >> 1) & 1][3]), \
         "i"(16 * (st)), "i"((((st) + 1) & 1) * RS) : "memory")
#define B_BAR(i) BARRIER()
    u32 wdum;
    u32 vtw; { const int l = lane < 49 ? lane : 48, r = (l * 37) >> 8; vtw = (u32)(14 * rr + 7 * (q >> 1) + r) * 3584 + (xcd & 3) * 896 + (l - 7 * r) * 128; PINV(vtw); }
    char* ws;
    u32 uo[5], wf[3];
    auto p2pre = [&]() AINL {
#pragma unroll
    for (int p = 0; p < 5; ++p) { const u32 s = lane + 64 * p; uo[p] = (s - ((s * 57) >> 9)) * 16 + 4096 - 1024 * (p & 3); pinu(uo[p]); }
    wf[0] = lbase + RB + (g4 & 1) * WB_UP + (8 * (g4 >> 1) + (i16 >> 2)) * 96 + 8 * (i16 & 3); wf[1] = wf[0] + RS; wf[2] = wf[1] + RS;
    pinu(wf[0]); pinu(wf[1]); pinu(wf[2]);
    };
    if (q < 2) {
      u32 vx[4], vw[2], vu[2], vb[3];
#pragma unroll
      for (int p = 0; p < 4; ++p) { vx[p] = (u32)(8 * p + (lane >> 3)) * 896 + (((lane & 7) ^ g4 ^ (4 * (p & 1))) * 16) + 4096 - 1024 * p; PINV(vx[p]); }
#pragma unroll
      for (int p = 0; p < 2; ++p) {
        vw[p] = (u32)(8 * p + (lane >> 3)) * 3584 + ((((lane & 7) >> 1) ^ (g4 & 1)) * 32) + (lane & 1) * 16 + 4096 - 1024 * p; PINV(vw[p]);
        vu[p] = vw[p] ^ 64; PINV(vu[p]); }
#pragma unroll
      for (int v = 0; v < 3; ++v) { const u32 s = 64 * v + lane, r = (s * 171) >> 10; const u32 c = s - 6 * r; vb[v] = r * 3584 + ((int)(c >> 1) < a0 ? c : c + 8) * 16 + 4096 - 1024 * v; PINV(vb[v]); }
      ws = P.ws;
      const char* xg = (const char*)P.x + (size_t)(128 * mt + 64 * q) * 896 - 4096;
      const char* wgp = (const char*)P.wg + (size_t)(32 * q) * 3584 + 224 * nt + 32 * a0 - 4096;
      const char* wup = (const char*)P.wu + (size_t)(32 * q) * 3584 + 224 * nt + 32 * a0 - 4096;
      const char* wbs = (const char*)(q ? P.wu : P.wg) + 224 * nt - 4096;
      auto stage_dma = [&](int st) AINL {
        int c = st + c0; c = c >= 7 ? c - 7 : c;
        const u32 sl = lbase + RB + ((st + 1) & 3) * RS;
#pragma unroll
        for (int i = 0; i < 2; ++i) {
          const char* px = xg + c * 128 + i * (32 * 896);
          setm0(sl + (2 * q + i) * 4096);
          dma<0>(vx[0], px); dma<1024>(vx[1], px); dma<2048>(vx[2], px); dma<3072>(vx[3], px);
          const u32 wo = (u32)(c * 64 + i * 16) * 3584u;
          setm0(sl + 16384 + (2 * q + i) * 2048);
          dma<0>(vw[0], wgp + wo); dma<1024>(vw[1], wgp + wo);
          setm0(sl + 16384 + 8192 + (2 * q + i) * 2048);
          dma<0>(vu[0], wup + wo); dma<1024>(vu[1], wup + wo);
        }
      };
      auto wb_dma = [&](int u0, int n, int slot) AINL {
#pragma unroll
        for (int i = 0; i < n; ++i) {
          int us = u0 + i + 2 * c0; us = us >= 14 ? us - 14 : us;
          const char* s_ = wbs + (u32)us * (32u * 3584u);
          setm0(lbase + RB + slot * RS + q * WB_UP + i * 3072); dma<0>(vb[0], s_); dma<1024>(vb[1], s_); dma<2048>(vb[2], s_);
        }
      };
      gld4(wdum, vtw, (q & 1) ? P.wu : P.wg);
      gld4_sc1(ep, 0, ws + WS_EPOCH);
      stage_dma(0); stage_dma(1); stage_dma(2);
      p2pre();
      WAITV(32);
      PINV(ep); PINV(wdum);
      BARRIER();
      __builtin_amdgcn_s_setprio(2);
      stage_dma(3); B_XREAD(0); WAITV(32); B_BAR(1);
      stage_dma(4); B_XREAD(1); WAITV(32); B_BAR(2);
      stage_dma(5); B_XREAD(2); WAITV(32); B_BAR(3);
      stage_dma(6); B_XREAD(3); WAITV(32); B_BAR(4);
      wb_dma(0, 5, 0); B_XREAD(4); WAITV(31); B_BAR(5);
      wb_dma(5, 5, 1); B_XREAD(5); WAITV(30); BARRIER();
      wb_dma(10, 4, 2); B_XREAD(6);
    } else {
      const int tc = tid & 127;
      u32 vpr = (u32)((q & 1) * 128 + lane * 2) * 2; PINV(vpr);
      ws = P.ws;
      const int ch2 = have2 ? b + 256 : b;
      gld4_sc1(ep, 0, ws + WS_EPOCH);
      {
        const u32 o = (u32)ch2 * 512u;
        gld4(pr[0], vpr, (const char*)P.Are + o); gld4(pr[1], vpr, (const char*)P.Aim + o); gld4(pr[2], vpr, (const char*)P.Bre + o);
        gld4(pr[3], vpr, (const char*)P.Bim + o); gld4(pr[4], vpr, (const char*)P.Cre + o); gld4(pr[5], vpr, (const char*)P.Cim + o);
        gld2(ldt, 0, (const char*)P.logdt + ch2 * 2);
      }
      gld4(wdum, vtw, (q & 1) ? P.wu : P.wg);
      WAITV(1);
      PINV(ep); PINV(pr[0]); PINV(pr[1]); PINV(pr[2]); PINV(pr[3]); PINV(pr[4]); PINV(pr[5]); PINV(ldt);
      T = __builtin_amdgcn_readfirstlane(ep) + 1u;
      if (have2) {
        const float dt = rbf(__builtin_amdgcn_exp2f(bf2f(ldt) * 1.4426950408889634f));
        f32x4 kacc[2][2];
        const u32 so = lbase + L_KS2 + (q & 1) * KS_SZ;
        setm0(so);
#pragma unroll
        for (int i = 0; i < 4; ++i) kacc[i >> 1][i & 1] = (f32x4){0.f, 0.f, 0.f, 0.f};
#pragma nounroll
        for (int u = 0; u < 2; ++u) {
          kgen<true, 3>(pr, dt, so, kacc, lane, [&](auto) AINL {});
#pragma unroll
          for (int i = 0; i < 6; ++i) pr[i] >>= 16;
        }
        kgen_out(kacc, so);
      }
      u32x4 stg, cg0, cg1;
      if (q == 3) gld16_sc1(stg, lane * 16, ws + WS_ST + (size_t)mt * 1024);
      BARRIER();
      __builtin_amdgcn_s_setprio(2);
      B_XREAD(0);
      u32x2 xt[2][4];
#pragma unroll
      for (int h = 0; h < 2; ++h)
#pragma unroll
        for (int p = 0; p < 4; ++p) {
          const int t = 64 * h + 32 * (p >> 1) + 8 * g4 + 4 * (p & 1) + (i16 >> 2);
          const int c8 = 4 * (nt & 1) + 2 * (q & 1) + ((i16 & 3) >> 1);
          xt[h][p] = trread(lds + RB + RS + t * 128 + ((c8 ^ ((t >> 1) & 7)) * 16) + (i16 & 1) * 8);
        }
      WAITV(0); PINV(wdum);
      if (q == 2) {
        float s = 0.f;
#pragma unroll
        for (int i = 0; i < 4; ++i) s += *(LAS const float*)(lds + L_CSP + (i * 16 + i16) * 4);
        const u32x2 g = {__float_as_uint(s), T};
        if (lane < 14)
          gst8_sc1<0>((u32)lane * 8, g, ws + WS_CS + (size_t)((mt >> 3) * 1792 + 112 * nt + 14 * (mt & 7)) * 8);
      } else {
        PINV(stg);
#pragma nounroll
        for (u32 s = 0; s < SPIN_CAP; ++s) {
          if (__all(stg.y == T && stg.w == T)) break;
          __builtin_amdgcn_s_sleep(1);
          asm volatile("global_load_dwordx4 %0, %1, %2 sc1\n\ts_waitcnt vmcnt(0)" : "=v"(stg) : "v"(lane * 16), "s"(ws + WS_ST + (size_t)mt * 1024) : "memory");
        }
        *(LAS u32x2*)(lds + L_ST + lane * 8) = (u32x2){stg.x, stg.z};
        const float r0 = __builtin_amdgcn_rcpf(bfhi(stg.x)), r1 = __builtin_amdgcn_rcpf(bfhi(stg.z));
        *(LAS f32x4*)(lds + L_ST2 + lane * 16) = (f32x4){r0, -bf2f(stg.x) * r0, r1, -bf2f(stg.z) * r1};
      }
      B_BAR(1);
      auto xnt = [&](int h) AINL {
        u32 o[8];
        LAS const char* sp = lds + L_ST2 + (64 * h + 8 * g4) * 8;
#pragma unroll
        for (int m = 0; m < 4; ++m) {
          const f32x4 sa = *(LAS const f32x4*)(sp + (m >> 1) * 256 + (m & 1) * 32), sb = *(LAS const f32x4*)(sp + (m >> 1) * 256 + (m & 1) * 32 + 16);
          o[2 * m] = pk_bf16(ffma1(bf2f(xt[h][m].x), sa[0], sa[1]), ffma1(bfhi(xt[h][m].x), sa[2], sa[3]));
          o[2 * m + 1] = pk_bf16(ffma1(bf2f(xt[h][m].y), sb[0], sb[1]), ffma1(bfhi(xt[h][m].y), sb[2], sb[3]));
        }
        u32x4 s1, s2;
#pragma unroll
        for (int k = 0; k < 4; ++k) {
          const u32 x = i16 < 8 ? o[4 + k] : o[k];
          const u32 y = (u32)__builtin_amdgcn_update_dpp(0, (int)x, 0x128, 0xf, 0xf, true);
          s1[k] = i16 < 8 ? o[k] : y; s2[k] = i16 < 8 ? y : o[4 + k];
        }
        if (nt < 14) {
          char* dst = ws + WS_XT + (size_t)(32 * nt + 16 * (q & 1)) * (XT_ROW * 2) + 128 + (128 * mt + 64 * h) * 2;
          const u32 vo = (u32)(i16 & 7) * (XT_ROW * 2) + (i16 >> 3) * 64 + g4 * 16;
          gst16o_sc1<0>(vo, s1, dst); gst16o_sc1<0>(vo, s2, dst + 8 * (XT_ROW * 2));
          if (mt == 0 && h == 0 && lane < 16) gst16o_sc1<-16>((u32)lane * (XT_ROW * 2), (u32x4){0, 0, 0, 0}, dst);
        }
      };
      B_XREAD(1); xnt(0); B_BAR(2);
      B_XREAD(2); xnt(1); B_BAR(3);
      B_XREAD(3);
      WAITV(0);
      if (lane < 2 && nt < 14) gst4_sc1(lane * 8, T, ws + WS_FX + (nt * 64 + mt * 4 + (q & 1)) * 4);
      if (q == 2) { gld16_sc1(cg0, vst, ws + WS_CS + (size_t)(112 * nt) * 8); gld16_sc1(cg1, vst, ws + WS_CS + (size_t)(1792 + 112 * nt) * 8); }
      B_BAR(4);
      B_XREAD(4); p2pre(); B_BAR(5);
      B_XREAD(5);
      if (q == 2) {
        WAITV(0); PINV(cg0); PINV(cg1);
#pragma nounroll
        for (u32 s = 0; s < SPIN_CAP; ++s) {
          if (__all(cg0.y == T && cg0.w == T && cg1.y == T && cg1.w == T)) break;
          __builtin_amdgcn_s_sleep(1);
          asm volatile("global_load_dwordx4 %0, %2, %3 sc1\n\tglobal_load_dwordx4 %1, %2, %4 sc1\n\ts_waitcnt vmcnt(0)" : "=&v"(cg0), "=&v"(cg1)
                       : "v"(vst), "s"(ws + WS_CS + (size_t)(112 * nt) * 8), "s"(ws + WS_CS + (size_t)(1792 + 112 * nt) * 8) : "memory");
        }
        *(LAS u32x2*)(lds + L_CS + (vst >> 1)) = (u32x2){cg0.x, cg0.z};
        *(LAS u32x2*)(lds + L_CS + 448 + (vst >> 1)) = (u32x2){cg1.x, cg1.z};
      }
      BARRIER();
      B_XREAD(6);
    }
    T = __builtin_amdgcn_readfirstlane(ep) + 1u;
    const int kch = ((q >> 1) && have2) ? b + 256 : b;
    const u32 stt = *(LAS const u32*)(lds + L_ST + (32 * q + t31) * 4);
    const float sd = bfhi(stt), rs = __builtin_amdgcn_rcpf(sd);
    float k0 = -2.3022081986f * rs, k1 = k0 * 0.044715f * rs * rs, s2 = sd * sd;
    const u32 nm = hh ? 0u : (((stt ^ 0x8000u) & 0xffffu) * 0x10001u);
    u32 nm2 = nm;
    u32 ca[3];
    u32 hvo[3];
#pragma unroll
    for (int j = 0; j < 3; ++j) {
      const int jt = j < a0 ? j : j + 4;
      const float c = *(LAS const float*)(lds + L_CS + ((g4 & 1) * 112 + 16 * jt + i16) * 4);
      const float chi = rbf(c);
      ca[j] = pk_bf16(chi, c - chi); PINV(ca[j]);
      hvo[j] = lane * 16 + jt * 1024;
    }
    u32 fxv;
    const char* urb = ws + WS_XT + (size_t)kch * (XT_ROW * 2) + ((q & 1) ? 124 : 128) - 4096;
    bool ugot = false;
    auto u_dma = [&]() AINL {
      setm0(lbase + L_U + q * U_SZ + 15 * 144);
      dma_sc1<0>(uo[0], urb); dma_sc1<1024>(uo[1], urb); dma_sc1<2048>(uo[2], urb); dma_sc1<3072>(uo[3], urb);
      setm0(lbase + L_U + q * U_SZ + 15 * 144 + 4096);
      dma_sc1<0>(uo[4], urb);
    };
    PINV(k0); PINV(k1); PINV(s2); PINV(nm2);
    char* hdst = ws + WS_HF + ((size_t)(4 * mt + q) * 112 + 7 * nt) * 1024;
    WAITV(12);
    BARRIER();
    __builtin_amdgcn_s_setprio(PR_B2);
    {
      f32x16 acc[3];
      constexpr int PD = 7;
      u32x2 fr[8][2];
#define P2_READ(n) do { LAS const char* p_ = (LAS const char*)(size_t)wf[((n) / 3) / 10] + (((n) / 3) % 10) * 1536 + ((n) % 3) * 32; \
        fr[(n) & 7][0] = trread(p_); fr[(n) & 7][1] = trread(p_ + 384); } while (0)
      static_for<0, PD>([&](auto ic) AINL { P2_READ(ic.value); });
      static_for<0, 84>([&](auto ic) AINL {
        constexpr int n = ic.value, j = n % 3, s = n / 3;
        if constexpr (n == B7B) { WAITV(0); asm volatile("s_barrier" ::: "memory"); }
        if constexpr (n == 14) {
          const u32x4 z = {0, 0, 0, 0};
          LAS char* ub = lds + L_U + q * U_SZ;
          *(LAS u32x4*)(ub + lane * 16) = z; *(LAS u32x4*)(ub + 1024 + lane * 16) = z;
          if (lane < 7) *(LAS u32x4*)(ub + 2048 + lane * 16) = z;
        }
        if constexpr (n == FXP) gld4_sc1(fxv, (u32)((kch >> 5) * 64 + lane) * 4, ws + WS_FX);
        if constexpr (n == FXC) { WAITV(0); PINV(fxv); ugot = __all(fxv == T); if (ugot) u_dma(); }
        if constexpr (n + PD < 84) P2_READ(n + PD);
        if constexpr (!(n & 1)) pin4(fr[n & 7][0], fr[n & 7][1], fr[(n + 1) & 7][0], fr[(n + 1) & 7][1]);
        p2_mmv<4 * s, s == 0>(acc[j], (u32x4){fr[n & 7][0].x, fr[n & 7][0].y, fr[n & 7][1].x, fr[n & 7][1].y});
        if constexpr (s == 27) mfma_corr(acc[j], (u32x4){ca[j], 0, 0, 0}, (u32x4){nm2, 0, 0, 0});
      });
      if (lane == 0) __hip_atomic_fetch_add((LAS u32*)(lds + L_FLG + 28), 1u, __ATOMIC_RELAXED, __HIP_MEMORY_SCOPE_WORKGROUP);
      pin(acc[0]); gst16o_sc1<0>(hvo[0], geglu8p(acc[0], k0, k1, s2), hdst);
      pin(acc[1]); gst16o_sc1<0>(hvo[1], geglu8p(acc[1], k0, k1, s2), hdst);
      pin(acc[2]); gst16o_sc1<0>(hvo[2], geglu8p(acc[2], k0, k1, s2), hdst);
    }
    WAITV(0);
    if (lane == 0) gst4_sc1(0, T, ws + WS_FH + ((4 * mt + q) * 32 + 16 + nt) * 4);
    if (!ugot) { poll_flags((u32)((kch >> 5) * 64 + lane) * 4, ws + WS_FX, T); u_dma(); WAITV(0); }
    bsync(lds + L_FLG + 16, lane);
#pragma nounroll
    for (u32 s = 0; s < (SPIN_CAP << 4); ++s) { if (*(LAS volatile u32*)(lds + L_FLG + 20) >= 4u) break; __builtin_amdgcn_s_sleep(1); }
    asm volatile("" ::: "memory");
    __builtin_amdgcn_s_setprio(PR_B3);
    const int cs = q >> 1, rho = q & 1;
    if (!cs || have2) {
      f32x16 yv[2];
      const int vv = g4 & 1;
      LAS const char* pa = lds + L_U + (2 * cs + vv) * U_SZ + 144 * (15 + i16 - hh - 8 * rho - 6);
      LAS const char* pk = lds + L_KI + cs * KI_CH + vv * KI_SZ + (4200 - 112 - 2048 - 768) - 8 * i16 - 128 * hh - 1024 * rho;
      constexpr int CD = 3;
      u32x4 ra0[CD], ra1[CD]; u32x2 rk[CD][4];
#define CV_LOAD(st) do { constexpr int gl_ = (st) >> 3, o_ = 16 * (7 - ((st) & 7)); \
      rk[(st) % CD][0] = *(LAS const volatile u32x2*)(pk + 256 * (3 - gl_) + o_ + 2048); \
      rk[(st) % CD][1] = *(LAS const volatile u32x2*)(pk + 256 * (3 - gl_) + o_ + 2048 + 8); \
      ra1[(st) % CD] = *(LAS const volatile u32x4*)(pa + 288 * (3 - gl_) + o_ + 2304); \
      ra0[(st) % CD] = *(LAS const volatile u32x4*)(pa + 288 * (3 - gl_) + o_); \
      rk[(st) % CD][2] = *(LAS const volatile u32x2*)(pk + 256 * (3 - gl_) + o_); \
      rk[(st) % CD][3] = *(LAS const volatile u32x2*)(pk + 256 * (3 - gl_) + o_ + 8); } while (0)
      CV_LOAD(0); CV_LOAD(1);
      static_for<0, 32>([&](auto ic) AINL {
        constexpr int st = ic.value;
        if constexpr (st + CD - 1 < 32) CV_LOAD(st + CD - 1);
        const u32x4 b0 = {rk[st % CD][0].x, rk[st % CD][0].y, rk[st % CD][1].x, rk[st % CD][1].y};
        const u32x4 b1 = {rk[st % CD][2].x, rk[st % CD][2].y, rk[st % CD][3].x, rk[st % CD][3].y};
        if constexpr (st == 0) { MFMA32_0(yv[1], ra1[st % CD], b0); MFMA32_0(yv[0], ra0[st % CD], b0); }
        else { MFMA32(yv[1], ra1[st % CD], b0); MFMA32(yv[0], ra0[st % CD], b0); }
        MFMA32(yv[1], ra0[st % CD], b1);
      });
      asm volatile("s_nop 7\n\ts_nop 7" : "+v"(yv[0]), "+v"(yv[1]));
      LAS f32x4* xc = (LAS f32x4*)(lds + L_XCH + q * 4096) + lane;
#pragma unroll
      for (int g = 0; g < 4; ++g) {
        f32x4 v;
#pragma unroll
        for (int e = 0; e < 4; ++e) v[e] = rho ? yv[1][4 * g + e] : yv[0][4 * g + e];
        xc[64 * g] = v;
      }
      asm volatile("s_waitcnt lgkmcnt(0)" ::: "memory");
      *(LAS volatile u32*)(lds + L_FLG + q * 4) = 1u;
#pragma nounroll
      for (u32 s = 0; s < (SPIN_CAP << 4); ++s) { if (*(LAS volatile u32*)(lds + L_FLG + (q ^ 1) * 4)) break; }
      asm volatile("" ::: "memory");
      LAS const f32x4* xr = (LAS const f32x4*)(lds + L_XCH + (q ^ 1) * 4096) + lane;
      float yy[16];
#pragma unroll
      for (int g = 0; g < 4; ++g) {
        const f32x4 v = xr[64 * g];
#pragma unroll
        for (int e = 0; e < 4; ++e) yy[4 * g + e] = fadd1(v[e], rho ? yv[0][4 * g + e] : yv[1][4 * g + e]);
      }
      char* arow = ws + WS_AT + (size_t)kch * 8192 + (rho ? 0 : 4096);
      const u32 aoff = hh * 1024 + vv * 2048 + i16 * 16;
      static_for<0, 4>([&](auto ic) AINL {
        constexpr int k = ic.value;
        const u32 p0 = pk_bf16(yy[k + 8], yy[k]), p1 = pk_bf16(yy[k + 12], yy[k + 4]);
        const auto sw = __builtin_amdgcn_permlane16_swap(p0, p1, false, false);
        gst16o_sc1<256 * k>(aoff, (u32x4){__builtin_amdgcn_perm(sw[0], sw[1], 0x05040100u), T, __builtin_amdgcn_perm(sw[0], sw[1], 0x07060302u), T}, arow);
      });
    }
    __builtin_amdgcn_s_setprio(0);
    if (b == 255 && w == 7) {
#pragma unroll 1
      for (int i = 0; i < 4; ++i) poll_flags((u32)((4 * i + g4) * 128 + i16) * 4, ws + WS_FH, T);
      if (lane == 0) gst4_sc1(0, T, ws + WS_EPOCH);
    }
    u32x4 xv;
    const int id = tid < 448 ? tid : 0;
    const u32 xo = (u32)(id / 7) * 896 + (id % 7) * 16;
    {
      gld16(xv, xo, (const char*)P.x + (size_t)mt2 * 64 * 896 + ct * 112);
      const char* ab = ws + WS_AT + (size_t)(56 * ct + 14 * q) * 8192 + mt2 * 256;
      const u32 o0 = (u32)g4 * 8192 + i16 * 16, o3 = (u32)(12 + (g4 & 1)) * 8192 + i16 * 16;
      u32x4 a0_, a1, a2, a3;
#pragma nounroll
      for (u32 s = 0; s < SPIN_CAP; ++s) {
        asm volatile("global_load_dwordx4 %0, %4, %6 sc1\n\tglobal_load_dwordx4 %1, %4, %7 sc1\n\tglobal_load_dwordx4 %2, %4, %8 sc1\n\t"
                     "global_load_dwordx4 %3, %5, %6 sc1\n\ts_waitcnt vmcnt(0)"
                     : "=&v"(a0_), "=&v"(a1), "=&v"(a2), "=&v"(a3) : "v"(o0), "v"(o3), "s"(ab), "s"(ab + 4 * 8192), "s"(ab + 8 * 8192) : "memory");
        const u32 bad = (a0_.y ^ T) | (a0_.w ^ T) | (a1.y ^ T) | (a1.w ^ T) | (a2.y ^ T) | (a2.w ^ T) | (a3.y ^ T) | (a3.w ^ T);
        if (__all(bad == 0u)) break;
        __builtin_amdgcn_s_sleep(1);
      }
      LAS char* p0 = lds + L_BASE + i16 * 4 * PSTR + (14 * q + g4) * 4;
      LAS char* p3 = lds + L_BASE + i16 * 4 * PSTR + (14 * q + 12 + (g4 & 1)) * 4;
      *(LAS float*)(p0) = bf2f(a0_.x); *(LAS float*)(p0 + PSTR) = bfhi(a0_.x); *(LAS float*)(p0 + 2 * PSTR) = bf2f(a0_.z); *(LAS float*)(p0 + 3 * PSTR) = bfhi(a0_.z);
      *(LAS float*)(p0 + 16) = bf2f(a1.x); *(LAS float*)(p0 + 16 + PSTR) = bfhi(a1.x); *(LAS float*)(p0 + 16 + 2 * PSTR) = bf2f(a1.z); *(LAS float*)(p0 + 16 + 3 * PSTR) = bfhi(a1.z);
      *(LAS float*)(p0 + 32) = bf2f(a2.x); *(LAS float*)(p0 + 32 + PSTR) = bfhi(a2.x); *(LAS float*)(p0 + 32 + 2 * PSTR) = bf2f(a2.z); *(LAS float*)(p0 + 32 + 3 * PSTR) = bfhi(a2.z);
      *(LAS float*)(p3) = bf2f(a3.x); *(LAS float*)(p3 + PSTR) = bfhi(a3.x); *(LAS float*)(p3 + 2 * PSTR) = bf2f(a3.z); *(LAS float*)(p3 + 3 * PSTR) = bfhi(a3.z);
    }
    BARRIER();
    PINV(xv);
    if (tid < 448) finish(ws, id, xo, xv);
  }
}

__global__ __launch_bounds__(512, 2) void fused(const Params P) {
  extern __shared__ __attribute__((aligned(16))) char lds_[];
  const int w = __builtin_amdgcn_readfirstlane(threadIdx.x >> 6);
  if (w >= 4) run<true>(P, (LAS char*)lds_, w); else run<false>(P, (LAS char*)lds_, w);
}

extern "C" void kernel_launch(void* const* d_in, const int* in_sizes, int n_in,
                              void* d_out, int out_size, void* d_ws, size_t ws_size,
                              hipStream_t stream) {
  Params P;
  P.x = (const u16*)d_in[0]; P.Are = (const u16*)d_in[1]; P.Aim = (const u16*)d_in[2]; P.Bre = (const u16*)d_in[3];
  P.Bim = (const u16*)d_in[4]; P.Cre = (const u16*)d_in[5]; P.Cim = (const u16*)d_in[6]; P.logdt = (const u16*)d_in[7];
  P.dskip = (const u16*)d_in[8]; P.wg = (const u16*)d_in[10]; P.wu = (const u16*)d_in[11]; P.wd = (const u16*)d_in[12];
  P.out = (u16*)d_out; P.ws = (char*)d_ws;
  (void)hipFuncSetAttribute((const void*)fused, hipFuncAttributeMaxDynamicSharedMemorySize, LDS_BYTES);
  fused<<<256, 512, LDS_BYTES, stream>>>(P);
}
```
